```python
import torch

def _softmax(x, axis=-1):
    return torch.softmax(x, dim=axis)
def _gelu(x):
    return torch.nn.functional.gelu(x, approximate="tanh")
def _silu(x):
    return torch.nn.functional.silu(x)
def _sigmoid(x):
    return torch.sigmoid(x)
def _layernorm(x, axis=-1, eps=1e-5):
    m = x.mean(dim=axis, keepdim=True)
    v = x.var(dim=axis, keepdim=True, unbiased=False)
    return (x - m) / torch.sqrt(v + eps)
def _scatter_add(tgt, idx, src, axis=0):
    return tgt.clone().index_add_(axis, idx, src)
def _segment_sum(src, seg, n):
    out = torch.zeros((n,) + src.shape[1:], dtype=src.dtype)
    return out.index_add_(0, seg, src)
def _pad(x, pw):
    flat = tuple(v for pair in reversed(pw) for v in pair)
    return torch.nn.functional.pad(x, flat)

kernel_name = 'parallel_sum__sliding_block_cached__swiglu_ffn__decode'

def setup_inputs():

    _w = (lambda a: (lambda t: t.to(torch.bfloat16) if t.is_floating_point() else t)(torch.as_tensor(a)))
    def _A_setup():
        B, Sq, D = 1, 1, 1024
        Hq, Hkv, Sk, Dh = 32, 4, 32768, 128
        assert Hq % Hkv == 0, (Hq, Hkv)
        return {
            "x": _w((lambda *s: torch.randn(*s, dtype=torch.bfloat16) * 0.05)(B, Sq, D)),
            "k_cache": _w((lambda *s: torch.randn(*s, dtype=torch.bfloat16) * 3.2)(B, Hkv, Sk, Dh)),
            "v_cache": _w((lambda *s: torch.randn(*s, dtype=torch.bfloat16) * 1.6)(B, Hkv, Sk, Dh)),
            "wq": _w((lambda *s: torch.randn(*s, dtype=torch.bfloat16) * 3.2)(D, Hq * Dh) * (D ** -0.5)),
            "wo": _w((lambda *s: torch.randn(*s, dtype=torch.bfloat16) * 1.6)(Hq * Dh, D) * ((Hq * Dh) ** -0.5)),
        }
    _A_in = _A_setup()

    def _B_setup():
        B, S, D, F = 1, 1, 1024, 3584
        return {
            "x": _w((lambda *s: torch.randn(*s, dtype=torch.bfloat16) * 0.05)(B, S, D)),
            "w_gate": _w((lambda *s: torch.randn(*s, dtype=torch.bfloat16) * 0.05)(D, F)),
            "w_up": _w((lambda *s: torch.randn(*s, dtype=torch.bfloat16) * 0.05)(D, F)),
            "w_down": _w((lambda *s: torch.randn(*s, dtype=torch.bfloat16) * 0.05)(F, D)),
        }
    _B_in = _B_setup()

    return {
        "A_x": _A_in["x"],
        "A_k_cache": _A_in["k_cache"],
        "A_v_cache": _A_in["v_cache"],
        "A_wq": _A_in["wq"],
        "A_wo": _A_in["wo"],
        "B_x": _B_in["x"],
        "B_w_gate": _B_in["w_gate"],
        "B_w_up": _B_in["w_up"],
        "B_w_down": _B_in["w_down"],
    }

def reference(A_x, A_k_cache, A_v_cache, A_wq, A_wo, B_x, B_w_gate, B_w_up, B_w_down):
    def _A_ref(x, k_cache, v_cache, wq, wo):
        B, Sq, D = x.shape
        Hq, Dh = 32, 128
        Hkv, Sk = k_cache.shape[1], k_cache.shape[2]
        W = min(4096, max(256, Sk // 4))
        rep = Hq // Hkv
        q = (lambda x, *d: x.permute(*d))((x @ wq).reshape(B, Sq, Hq, Dh), 0, 2, 1, 3)
        k = (lambda x, n, axis: torch.repeat_interleave(x, n, dim=axis))(k_cache[:, :, Sk - W:, :], rep, axis=1)
        v = (lambda x, n, axis: torch.repeat_interleave(x, n, dim=axis))(v_cache[:, :, Sk - W:, :], rep, axis=1)
        scores = torch.einsum("bhqd,bhkd->bhqk", q, k) * (Dh ** -0.5)
        p = _softmax(scores, axis=-1)
        o = torch.einsum("bhqk,bhkd->bhqd", p, v)
        return (lambda x, *d: x.permute(*d))(o, 0, 2, 1, 3).reshape(B, Sq, Hq * Dh) @ wo

    def _B_ref(x, w_gate, w_up, w_down):
        g = _silu(x @ w_gate)
        u = x @ w_up
        return (g * u) @ w_down

    x = A_x
    xn = _layernorm(x, axis=-1)
    return x + _A_ref(xn, A_k_cache, A_v_cache, A_wq, A_wo) + _B_ref(xn, B_w_gate, B_w_up, B_w_down)
```

```cpp
#include <hip/hip_runtime.h>
#include <hip/hip_bf16.h>
#include <stdint.h>
#define MIMIC 1
typedef unsigned short u16;
typedef unsigned v4u __attribute__((ext_vector_type(4)));
constexpr int D = 1024, HQ = 32, HKV = 4, SK = 32768, DH = 128, W = 4096, F = 3584;
__device__ __forceinline__ float bf2f(unsigned b) { return __builtin_bit_cast(float, b << 16); }
__device__ __forceinline__ float rbf(float f) {
    unsigned u = __builtin_bit_cast(unsigned, f); u += 0x7fffu + ((u >> 16) & 1u); u &= 0xffff0000u; return __builtin_bit_cast(float, u);
}
#define RB(x) rbf(x)
__device__ void layernorm(const u16* x, float* xn, float* red) {
    int t = threadIdx.x;
    float v[4]; float s = 0.f;
    for (int i = 0; i < 4; ++i) { v[i] = bf2f(x[t * 4 + i]); s += v[i]; }
    red[t] = s; __syncthreads();
    for (int o = 128; o > 0; o >>= 1) { if (t < o) red[t] += red[t + o]; __syncthreads(); }
    float mean = red[0] / D; __syncthreads();
    float q = 0.f; for (int i = 0; i < 4; ++i) { float d = v[i] - mean; q += d * d; }
    red[t] = q; __syncthreads();
    for (int o = 128; o > 0; o >>= 1) { if (t < o) red[t] += red[t + o]; __syncthreads(); }
    float var = red[0] / D; __syncthreads();
    float mb = RB(mean), vb = RB(var);
    float den = RB(sqrtf(RB(vb + 1e-5f)));
    for (int i = 0; i < 4; ++i) xn[t * 4 + i] = RB(RB(v[i] - mb) / den);
    __syncthreads();
}
__global__ void __launch_bounds__(256) k1(const u16* x, const u16* wq, const u16* wg, const u16* wu, float* q, float* h) {
    __shared__ float xn[D]; __shared__ float red[256]; __shared__ float part[32][64]; __shared__ float part2[32][64];
    layernorm(x, xn, red);
    int t = threadIdx.x, cg = t & 7, rg = t >> 3; int b = blockIdx.x;
    if (b < 64) {
        float acc[8] = {0};
        for (int r = rg; r < D; r += 32) {
            v4u w = *(const v4u*)(wq + (size_t)r * 4096 + b * 64 + cg * 8);
            float xv = xn[r];
            for (int j = 0; j < 4; ++j) { acc[2 * j] += xv * bf2f(w[j] & 0xffff); acc[2 * j + 1] += xv * bf2f(w[j] >> 16); }
        }
        for (int j = 0; j < 8; ++j) part[rg][cg * 8 + j] = acc[j];
        __syncthreads();
        if (t < 64) { float s = 0; for (int r = 0; r < 32; ++r) s += part[r][t]; q[b * 64 + t] = RB(s); }
    } else {
        b -= 64;
        float acc[8] = {0}, acc2[8] = {0};
        for (int r = rg; r < D; r += 32) {
            v4u w = *(const v4u*)(wg + (size_t)r * F + b * 64 + cg * 8);
            v4u w2 = *(const v4u*)(wu + (size_t)r * F + b * 64 + cg * 8);
            float xv = xn[r];
            for (int j = 0; j < 4; ++j) { acc[2 * j] += xv * bf2f(w[j] & 0xffff); acc[2 * j + 1] += xv * bf2f(w[j] >> 16);
                acc2[2 * j] += xv * bf2f(w2[j] & 0xffff); acc2[2 * j + 1] += xv * bf2f(w2[j] >> 16); }
        }
        for (int j = 0; j < 8; ++j) { part[rg][cg * 8 + j] = acc[j]; part2[rg][cg * 8 + j] = acc2[j]; }
        __syncthreads();
        if (t < 64) { float s = 0, s2 = 0; for (int r = 0; r < 32; ++r) { s += part[r][t]; s2 += part2[r][t]; }
            float g = RB(s), u = RB(s2); float sg = RB(g / (1.f + __expf(-g))); h[b * 64 + t] = RB(sg * u); }
    }
}
__global__ void __launch_bounds__(256) k2(const u16* kc, const u16* vc, const float* q, float* pm, float* pl, float* po) {
    __shared__ float qs[8][128]; __shared__ float sc[8][64]; __shared__ float ml[8][2];
    int t = threadIdx.x, sp = blockIdx.x, g = blockIdx.y;
    for (int i = t; i < 1024; i += 256) qs[i >> 7][i & 127] = q[g * 1024 + i];
    __syncthreads();
    const u16* kb = kc + ((size_t)g * SK + (SK - W) + sp * 64) * DH;
    const u16* vb = vc + ((size_t)g * SK + (SK - W) + sp * 64) * DH;
    for (int i = t; i < 512; i += 256) {
        int hh = i >> 6, key = i & 63; float s = 0;
        for (int d = 0; d < 128; ++d) s += qs[hh][d] * bf2f(kb[key * 128 + d]);
        sc[hh][key] = RB(RB(s) * 0.08838834764831845f);
    }
    __syncthreads();
    if (t < 8) { float m = -INFINITY; for (int k = 0; k < 64; ++k) m = fmaxf(m, sc[t][k]); float l = 0; for (int k = 0; k < 64; ++k) { float p = __expf(sc[t][k] - m); sc[t][k] = p; l += p; } ml[t][0] = m; ml[t][1] = l;
        pm[(g * 8 + t) * 64 + sp] = m; pl[(g * 8 + t) * 64 + sp] = l; }
    __syncthreads();
    for (int i = t; i < 1024; i += 256) {
        int hh = i >> 7, d = i & 127; float o = 0;
        for (int k = 0; k < 64; ++k) o += sc[hh][k] * bf2f(vb[k * 128 + d]);
        po[((size_t)(g * 8 + hh) * 64 + sp) * 128 + d] = o;
    }
}
__global__ void __launch_bounds__(128) k3(const float* pm, const float* pl, const float* po, float* o) {
    int hh = blockIdx.x, d = threadIdx.x;
    float M = -INFINITY; for (int s = 0; s < 64; ++s) M = fmaxf(M, pm[hh * 64 + s]);
    float L = 0, acc = 0; for (int s = 0; s < 64; ++s) { float w = __expf(pm[hh * 64 + s] - M); L += w * pl[hh * 64 + s]; acc += w * po[((size_t)hh * 64 + s) * 128 + d]; }
    o[hh * 128 + d] = RB(acc / L);
}
__global__ void __launch_bounds__(256) k4(const u16* x, const u16* wo, const u16* wd, const float* o, const float* h, u16* out) {
    __shared__ float part[32][64]; __shared__ float part2[32][64];
    int t = threadIdx.x, cg = t & 7, rg = t >> 3; int b = blockIdx.x;
    float acc[8] = {0}, acc2[8] = {0};
    for (int r = rg; r < 4096; r += 32) { v4u w = *(const v4u*)(wo + (size_t)r * D + b * 64 + cg * 8); float xv = o[r];
        for (int j = 0; j < 4; ++j) { acc[2 * j] += xv * bf2f(w[j] & 0xffff); acc[2 * j + 1] += xv * bf2f(w[j] >> 16); } }
    for (int r = rg; r < F; r += 32) { v4u w = *(const v4u*)(wd + (size_t)r * D + b * 64 + cg * 8); float xv = h[r];
        for (int j = 0; j < 4; ++j) { acc2[2 * j] += xv * bf2f(w[j] & 0xffff); acc2[2 * j + 1] += xv * bf2f(w[j] >> 16); } }
    for (int j = 0; j < 8; ++j) { part[rg][cg * 8 + j] = acc[j]; part2[rg][cg * 8 + j] = acc2[j]; }
    __syncthreads();
    if (t < 64) { float s = 0, s2 = 0; for (int r = 0; r < 32; ++r) { s += part[r][t]; s2 += part2[r][t]; }
        float xv = bf2f(x[b * 64 + t]);
        float r = rbf(RB(xv + RB(s)) + RB(s2));
        out[b * 64 + t] = (u16)(__builtin_bit_cast(unsigned, r) >> 16); }
}
extern "C" void kernel_launch(void* const* d_in, const int* in_sizes, int n_in, void* d_out, int out_size, void* d_ws, size_t ws_size, hipStream_t stream) {
    const u16* x = (const u16*)d_in[0]; const u16* kc = (const u16*)d_in[1]; const u16* vc = (const u16*)d_in[2];
    const u16* wq = (const u16*)d_in[3]; const u16* wo = (const u16*)d_in[4];
    const u16* wg = (const u16*)d_in[6]; const u16* wu = (const u16*)d_in[7]; const u16* wd = (const u16*)d_in[8];
    float* ws = (float*)d_ws;
    float* q = ws; float* h = ws + 4096; float* o = ws + 8192; float* pm = ws + 12288; float* pl = pm + 2048; float* po = pl + 2048;
    k1<<<120, 256, 0, stream>>>(x, wq, wg, wu, q, h);
    k2<<<dim3(64, 4), 256, 0, stream>>>(kc, vc, q, pm, pl, po);
    k3<<<32, 128, 0, stream>>>(pm, pl, po, o);
    k4<<<16, 256, 0, stream>>>(x, wo, wd, o, h, (u16*)d_out);
}
```

```cpp
#include <hip/hip_runtime.h>
#include <hip/hip_bf16.h>
#include <stdint.h>

#define LAS __attribute__((address_space(3)))
#define GAS __attribute__((address_space(1)))
typedef unsigned short u16;
typedef unsigned v4u __attribute__((ext_vector_type(4)));
typedef unsigned v2u __attribute__((ext_vector_type(2)));
typedef int v4i __attribute__((ext_vector_type(4)));
typedef float f32x4 __attribute__((ext_vector_type(4)));
typedef short s16x4 __attribute__((ext_vector_type(4)));
typedef __bf16 bf16x8 __attribute__((ext_vector_type(8)));
typedef __bf16 bf16x2 __attribute__((ext_vector_type(2)));

constexpr int DM = 1024, SK = 32768, WIN = 4096, FF = 3584;
constexpr unsigned WS_EPOCH = 0;
constexpr unsigned WS_S1 = 4096;
constexpr unsigned WS_SH = WS_S1 + 4 * 4096 * 4;
constexpr unsigned WS_S2O = WS_SH + 8 * 3584 * 8;
constexpr unsigned WS_S2M = WS_S2O + 128 * 64 * 128;
constexpr unsigned WS_S3 = WS_S2M + 32 * 64 * 16;
constexpr unsigned WS_END = WS_S3 + 128 * 1024 * 4;
constexpr unsigned L_A = 0;
constexpr unsigned L_K = 32768;
constexpr unsigned L_V = 49152;
constexpr unsigned L_C = 65536;
constexpr unsigned L_D = 98304;
constexpr unsigned L_XN = 131072;
constexpr unsigned L_Q = L_XN + 2048;
constexpr unsigned L_SC = L_Q + 8 * 272;
constexpr unsigned L_O = L_SC + 8 * 272;
constexpr unsigned L_H = L_O + 64;
constexpr unsigned L_FLAG = L_H + 64;
constexpr unsigned L_JUNK = L_FLAG + 64;
constexpr unsigned LDS_BYTES = L_JUNK + 4096;
enum { F_PROG = 0, F_XN = 1, F_TAG = 2, F_WQDONE = 3, F_GDONE = 4, F_QCNT = 5, F_SCNT = 6, F_APUB = 7, F_O = 8, F_H = 9, F_PUB = 10 };

#define D_EARLY 32
#define D_MID 24
#define D_LATE 24
#define DRYRUN 0
#define S1_AT 56
#define SH_AT 8
#define S2_SLEEP 16
constexpr unsigned LDS_SPIN = 1u << 20, GLB_SPIN = 1u << 16;
constexpr int AUX_SC1 = 16;
constexpr float LOG2E = 1.4426950408889634f;

__device__ __forceinline__ float bflo(unsigned u) { return __builtin_bit_cast(float, u << 16); }
__device__ __forceinline__ float bfhi(unsigned u) { return __builtin_bit_cast(float, u & 0xffff0000u); }
__device__ __forceinline__ float rbf(float f) { return (float)(__bf16)f; }
__device__ __forceinline__ unsigned pkbf(float lo, float hi) { bf16x2 v = {(__bf16)lo, (__bf16)hi}; return __builtin_bit_cast(unsigned, v); }
__device__ __forceinline__ unsigned fu(float f) { return __builtin_bit_cast(unsigned, f); }
__device__ __forceinline__ float uf(unsigned u) { return __builtin_bit_cast(float, u); }
__device__ __forceinline__ unsigned g_lo(float a, float b) { return __builtin_amdgcn_perm(fu(b), fu(a), 0x05030201u); }
__device__ __forceinline__ unsigned g_hi(float b, unsigned tagw) { return __builtin_amdgcn_perm(tagw, fu(b), 0x07060302u); }
__device__ __forceinline__ v4u pack4(float a, float b, float c, float d, unsigned tagw) { return v4u{g_lo(a, b), g_hi(b, tagw), g_lo(c, d), g_hi(d, tagw)}; }
__device__ __forceinline__ float g_a(unsigned lo) { return uf(lo << 8); }
__device__ __forceinline__ float g_b(unsigned lo, unsigned hi) { return uf(__builtin_amdgcn_perm(hi, lo, 0x0504030cu)); }
__device__ __forceinline__ bool tag_ok(v4u r, unsigned tagw) { return (((r.y ^ tagw) | (r.w ^ tagw)) >> 16) == 0u; }

template <int CTRL, int RM> __device__ __forceinline__ float dpp0(float v) {
    return __builtin_bit_cast(float, __builtin_amdgcn_update_dpp(0, __builtin_bit_cast(int, v), CTRL, RM, 0xf, true));
}
__device__ __forceinline__ float wave_sum(float v) {
    v += dpp0<0x111, 0xf>(v); v += dpp0<0x112, 0xf>(v); v += dpp0<0x114, 0xf>(v); v += dpp0<0x118, 0xf>(v);
    v += __builtin_bit_cast(float, __builtin_amdgcn_update_dpp(0, __builtin_bit_cast(int, v), 0x142, 0xa, 0xf, false));
    v += __builtin_bit_cast(float, __builtin_amdgcn_update_dpp(0, __builtin_bit_cast(int, v), 0x143, 0xc, 0xf, false));
    return uf(__builtin_amdgcn_readlane(fu(v), 63));
}
template <int CTRL> __device__ __forceinline__ float dppm(float v) {
    return __builtin_bit_cast(float, __builtin_amdgcn_mov_dpp(__builtin_bit_cast(int, v), CTRL, 0xf, 0xf, true));
}
__device__ __forceinline__ float xrow_max(float x) {
    auto s = __builtin_amdgcn_permlane16_swap(fu(x), fu(x), false, false);
    x = fmaxf(uf(s[0]), uf(s[1]));
    auto t = __builtin_amdgcn_permlane32_swap(fu(x), fu(x), false, false);
    return fmaxf(uf(t[0]), uf(t[1]));
}
__device__ __forceinline__ float xrow_sum(float x) {
    auto s = __builtin_amdgcn_permlane16_swap(fu(x), fu(x), false, false);
    x = uf(s[0]) + uf(s[1]);
    auto t = __builtin_amdgcn_permlane32_swap(fu(x), fu(x), false, false);
    return uf(t[0]) + uf(t[1]);
}
__device__ __forceinline__ float wave_max(float x) {
    x = fmaxf(x, dppm<0xB1>(x)); x = fmaxf(x, dppm<0x4E>(x)); x = fmaxf(x, dppm<0x141>(x)); x = fmaxf(x, dppm<0x128>(x));
    return xrow_max(x);
}
__device__ __forceinline__ f32x4 mfma(v4u a, v4u b, f32x4 c) {
    return __builtin_amdgcn_mfma_f32_16x16x32_bf16(__builtin_bit_cast(bf16x8, a), __builtin_bit_cast(bf16x8, b), c, 0, 0, 0);
}
__device__ __forceinline__ v2u trread(LAS unsigned char* L, unsigned off) {
    return __builtin_bit_cast(v2u, __builtin_amdgcn_ds_read_tr16_b64_v4i16((LAS s16x4*)(L + off)));
}
__device__ __forceinline__ v4u trfrag(LAS unsigned char* L, unsigned off, unsigned toff) {
    const v2u a = trread(L, off), b = trread(L, off + toff);
    return v4u{a.x, a.y, b.x, b.y};
}
__device__ __forceinline__ v4u xfrag(LAS unsigned char* L, unsigned off) {
    const v2u a = *(LAS v2u*)(L + off), b = *(LAS v2u*)(L + off + 32);
    return v4u{a.x, a.y, b.x, b.y};
}
#define CBAR() asm volatile("" ::: "memory")
#define LGKM0() asm volatile("s_waitcnt lgkmcnt(0)" ::: "memory")
template <bool SLEEP = true> __device__ __forceinline__ void wait_ge(volatile LAS unsigned* w, unsigned want) {
#pragma nounroll
    for (unsigned s = 0; s < LDS_SPIN; ++s) {
        if ((unsigned)__builtin_amdgcn_readfirstlane(*w) >= want) break;
        if (SLEEP) __builtin_amdgcn_s_sleep(1);
    }
    CBAR();
}
#define SIGNAL(flag) asm volatile("s_waitcnt lgkmcnt(0)\n\tds_add_u32 %0, %1 offset:%2" ::"v"((lane == 0 && !dry) ? L_FLAG : L_JUNK + 4u * lane), "v"(1u), "i"(4 * (flag)) : "memory")
__device__ __forceinline__ v4i mkrsrc(const void* p, unsigned bytes) {
    const uint64_t a = (uint64_t)p;
    return v4i{(int)(unsigned)a, (int)((unsigned)(a >> 32) & 0xffffu), (int)bytes, 0x00020000};
}
__device__ __forceinline__ void dma(v4i rs, unsigned voff, unsigned soff, unsigned lds) {
    asm volatile("s_mov_b32 m0, %0\n\ts_nop 0\n\tbuffer_load_dwordx4 %1, %2, %3 offen nt lds" ::"s"(lds), "v"(voff), "s"(rs), "s"(soff) : "memory");
}
#define VMWAIT(n) asm volatile("s_waitcnt vmcnt(%0)" ::"i"(n) : "memory")
#define LD(voff, soff) __builtin_amdgcn_raw_buffer_load_b128(wsr, (voff), (soff), AUX_SC1)
#define ST(v, voff, base) __builtin_amdgcn_raw_buffer_store_b128((v), wsr, (voff) + (base), 0, AUX_SC1)

#define STAMP(i) do { } while (0)
#define STAMP_FLUSH() do { } while (0)
#define PASSES(dry) { constexpr bool dry = false;
#define END_PASSES }

template <int DEP>
__device__ __forceinline__ void dma4(v4i rs, unsigned v0, unsigned v1, unsigned v2, unsigned v3, unsigned& soff, unsigned sstep, unsigned lds) {
    asm volatile("s_mov_b32 m0, %[lds]\n\t"
                 "s_nop 0\n\t"
                 "buffer_load_dwordx4 %[v0], %[rs], %[so] offen nt lds\n\t"
                 "s_add_u32 m0, m0, 0x400\n\t"
                 "s_add_u32 %[so], %[so], %[st]\n\t"
                 "buffer_load_dwordx4 %[v1], %[rs], %[so] offen nt lds\n\t"
                 "s_add_u32 m0, m0, 0x400\n\t"
                 "s_add_u32 %[so], %[so], %[st]\n\t"
                 "s_waitcnt vmcnt(%[dep])\n\t"
                 "buffer_load_dwordx4 %[v2], %[rs], %[so] offen nt lds\n\t"
                 "s_add_u32 m0, m0, 0x400\n\t"
                 "s_add_u32 %[so], %[so], %[st]\n\t"
                 "buffer_load_dwordx4 %[v3], %[rs], %[so] offen nt lds\n\t"
                 "s_add_u32 %[so], %[so], %[st]\n\t"
                 "s_waitcnt vmcnt(%[dep])"
                 : [so] "+s"(soff)
                 : [lds] "s"(lds), [v0] "v"(v0), [v1] "v"(v1), [v2] "v"(v2), [v3] "v"(v3), [rs] "s"(rs), [st] "s"(sstep), [dep] "i"(DEP)
                 : "memory", "scc");
}
template <int KSH, int DEP>
__device__ __forceinline__ void stream(const void* base, unsigned bytes, unsigned voff, unsigned soff, unsigned sstep, unsigned lds, int g0, int g1,
                                       int& prog, unsigned progaddr, bool dry) {
    const v4i rs = mkrsrc(base, dry ? 0u : bytes);
    unsigned end = lds + (g1 - g0) * 4096;
    if (dry) { lds = L_JUNK; end = lds + 4096; }
    unsigned v0 = voff, v1 = voff ^ ((1u << KSH) & 0xE0u), v2 = voff ^ ((2u << KSH) & 0xE0u), v3 = voff ^ ((3u << KSH) & 0xE0u);
#pragma nounroll
    do {
        dma4<DEP>(rs, v0, v1, v2, v3, soff, sstep, lds);
        lds += 4096; prog += 4;
        if (KSH == 5) { v0 ^= 0x80u; v1 ^= 0x80u; v2 ^= 0x80u; v3 ^= 0x80u; }
        if (DEP < 63) asm volatile("ds_max_i32 %0, %1" ::"v"(progaddr), "v"(prog - DEP) : "memory");
    } while (lds != end);
}

__global__ void __launch_bounds__(1024, 4)
fused_decode(const u16* __restrict__ x, const u16* __restrict__ kc, const u16* __restrict__ vc, const u16* __restrict__ wq, const u16* __restrict__ wo,
             const u16* __restrict__ wg, const u16* __restrict__ wu, const u16* __restrict__ wd, u16* __restrict__ out, unsigned char* ws) {
    extern __shared__ __attribute__((aligned(16))) unsigned char smem[];
    LAS unsigned char* L = (LAS unsigned char*)smem;
    volatile LAS unsigned* FL = (volatile LAS unsigned*)(L + L_FLAG);
    const int tid = threadIdx.x, lane = tid & 63, wave = __builtin_amdgcn_readfirstlane(tid >> 6), b = blockIdx.x;
    const int xc = b & 7, bi = b >> 3;
    const int gkv = xc >> 1, split = ((xc & 1) << 5) | bi;
    const int ks = xc * 16 + (bi >> 1), ct2 = bi & 1;
    const bool has1 = b < 224;
    const unsigned pbase = has1 ? 128u : 64u;
    const int lj = lane & 15, lg = lane >> 4;
    STAMP(0);

    if (__builtin_expect(wave == 0 || (DRYRUN && wave == 3), 1)) {
        const bool dry = DRYRUN && wave != 0;
        const unsigned pa = (lane == 0 && !dry) ? L_FLAG + 4 * F_PROG : L_JUNK + 4 * lane;
        int cnt = 0;
        asm volatile("" : "+v"(cnt));
        {
            const int rl = lane >> 3, p = lane & 7;
            const unsigned voff = rl * 8192 + (((p >> 1) ^ ((rl >> 1) & 3)) << 5) + (p & 1) * 16;
            const unsigned so = (unsigned)(b >> 6) * 256 * 8192 + (b & 63) * 128;
            stream<8, 63>(wq, DM * 4096 * 2, voff, so, 8 * 8192, L_A, 0, 2, cnt, pa, dry);
            __builtin_amdgcn_s_barrier();
            stream<8, D_EARLY>(wq, DM * 4096 * 2, voff, so + 64 * 8192, 8 * 8192, L_A + 8192, 2, 8, cnt, pa, dry);
        }
        STAMP(1);
        const size_t kvoff = ((size_t)gkv * SK + (SK - WIN) + split * 64) * 128;
        stream<6, D_EARLY>(kc + kvoff, 16384, lg * 256 + ((lj ^ lg) << 4), 0, 1024, L_K, 0, 4, cnt, pa, dry);
        const unsigned v256 = (((lj >> 1) ^ lg) << 5) + (lj & 1) * 16;
        stream<7, D_EARLY>(vc + kvoff, 16384, lg * 256 + v256, 0, 1024, L_V, 0, 4, cnt, pa, dry);
        STAMP(2);
        if (has1) {
            const int ct = b % 28, k8 = b / 28;
            const unsigned so = (unsigned)k8 * 128 * 7168 + ct * 256;
            stream<7, D_MID>(wg, DM * FF * 2, lg * 7168 + v256, so, 4 * 7168, L_C, 0, 8, cnt, pa, dry);
            stream<7, D_MID>(wu, DM * FF * 2, lg * 7168 + v256, so, 4 * 7168, L_D, 0, 8, cnt, pa, dry);
        } else {
            VMWAIT(24);
            asm volatile("ds_max_i32 %0, %1" ::"v"(pa), "v"(cnt - 24) : "memory");
        }
        STAMP(3);
        wait_ge(FL + F_WQDONE, dry ? 0 : 4);
        stream<5, D_LATE>(wo, 4096 * DM * 2, lane * 16, (unsigned)ks * 32 * 2048 + ct2 * 1024, 2048, L_A, 0, 8, cnt, pa, dry);
        wait_ge(FL + F_GDONE, (has1 && !dry) ? 8 : 0);
        stream<5, D_LATE>(wd, FF * DM * 2, lane * 16, (unsigned)ks * 28 * 2048 + ct2 * 1024, 2048, L_C, 0, 7, cnt, pa, dry);
        STAMP(4);
        VMWAIT(0);
        asm volatile("ds_max_i32 %0, %1" ::"v"(pa), "v"(cnt) : "memory");
        STAMP(5);
        if (!dry) { STAMP_FLUSH(); return; }
    }
    if (wave == 1) {
        const v4u xa = ((const GAS v4u*)x)[lane], xb = ((const GAS v4u*)x)[64 + lane];
        const unsigned ep = __hip_atomic_load((GAS unsigned*)(ws + WS_EPOCH), __ATOMIC_RELAXED, __HIP_MEMORY_SCOPE_AGENT);
        if (lane < 16) FL[lane] = 0u;
        LGKM0();
        __builtin_amdgcn_s_barrier();
        float v[16];
#pragma unroll
        for (int i = 0; i < 4; ++i) { v[2 * i] = bflo(xa[i]); v[2 * i + 1] = bfhi(xa[i]); v[8 + 2 * i] = bflo(xb[i]); v[9 + 2 * i] = bfhi(xb[i]); }
        float s = 0.f;
#pragma unroll
        for (int i = 0; i < 16; ++i) s += v[i];
        const float mean = wave_sum(s) * (1.0f / DM);
        float q = 0.f;
#pragma unroll
        for (int i = 0; i < 16; ++i) { const float d = v[i] - mean; q = fmaf(d, d, q); }
        const float var = wave_sum(q) * (1.0f / DM);
        const float mb = rbf(mean), den = rbf(__builtin_amdgcn_sqrtf(rbf(rbf(var) + 1e-5f)));
        const float rden = __builtin_amdgcn_rcpf(den);
        v4u oa, ob;
#pragma unroll
        for (int i = 0; i < 4; ++i) {
            oa[i] = pkbf(rbf(v[2 * i] - mb) * rden, rbf(v[2 * i + 1] - mb) * rden);
            ob[i] = pkbf(rbf(v[8 + 2 * i] - mb) * rden, rbf(v[9 + 2 * i] - mb) * rden);
        }
        *(LAS v4u*)(L + L_XN + lane * 16) = oa;
        *(LAS v4u*)(L + L_XN + 1024 + lane * 16) = ob;
        const unsigned tag = ep + 1u, tagw = tag << 16;
        if (lane == 0) FL[F_TAG] = tag;
        LGKM0();
        if (lane == 0) FL[F_XN] = 1u;
        STAMP(1);
        PASSES(dry)
        const __amdgpu_buffer_rsrc_t wsr = __builtin_amdgcn_make_buffer_rsrc((void*)ws, 0, dry ? 0 : (int)WS_END, 0x00020000);
        wait_ge(FL + F_PROG, dry ? 0 : pbase + SH_AT);
        STAMP(2);
        const unsigned off = (lg * FF + ks * 28 + (lj < 14 ? lj : 13) * 2) * 8;
        v4u r0, r1;
        bool ok0 = false, ok1 = false;
#pragma nounroll
        for (unsigned sp = 0; sp < GLB_SPIN; ++sp) {
            if (!ok0) r0 = LD(off, WS_SH);
            if (!ok1) r1 = LD(off, WS_SH + 4 * FF * 8);
            ok0 = tag_ok(r0, tagw); ok1 = tag_ok(r1, tagw);
            if (__all(ok0 & ok1) || dry) break;
            __builtin_amdgcn_s_sleep(2);
            CBAR();
        }
        float hv[2];
#pragma unroll
        for (int c = 0; c < 2; ++c) {
            const float g = rbf(xrow_sum(g_a(r0[2 * c]) + g_a(r1[2 * c])));
            const float u = rbf(xrow_sum(g_b(r0[2 * c], r0[2 * c + 1]) + g_b(r1[2 * c], r1[2 * c + 1])));
            hv[c] = rbf(g * __builtin_amdgcn_rcpf(1.0f + __builtin_amdgcn_exp2f(-g * LOG2E))) * u;
        }
        *(LAS unsigned*)(L + ((lane < 16 && !dry) ? L_H : L_JUNK) + lane * 4) = lj < 14 ? pkbf(hv[0], hv[1]) : 0u;
        SIGNAL(F_H);
        STAMP(3);
        END_PASSES
        STAMP_FLUSH();
        return;
    }
    if (wave != 3 || !DRYRUN) __builtin_amdgcn_s_barrier();
    if (wave == 2) {
        const int own = xc * 32 + bi;
        const v2u xv = *(const GAS v2u*)(x + own * 4);
        PASSES(dry)
        const __amdgpu_buffer_rsrc_t wsr = __builtin_amdgcn_make_buffer_rsrc((void*)ws, 0, dry ? 0 : (int)WS_END, 0x00020000);
        wait_ge(FL + F_PUB, dry ? 0 : 8);
        const unsigned tag = FL[F_TAG], tagw = tag << 16;
        STAMP(1);
        v4u r0, r1;
        bool ok0 = false, ok1 = false;
#pragma nounroll
        for (unsigned sp = 0; sp < GLB_SPIN; ++sp) {
            if (!ok0) r0 = LD(lane * 4096 + own * 16, WS_S3);
            if (!ok1) r1 = LD(lane * 4096 + own * 16, WS_S3 + 64 * 4096);
            ok0 = tag_ok(r0, tagw); ok1 = tag_ok(r1, tagw);
            if (__all(ok0 & ok1) || dry) break;
            CBAR();
        }
        STAMP(2);
        const float s0 = wave_sum(g_a(r0.x) + g_a(r1.x)), s1 = wave_sum(g_b(r0.x, r0.y) + g_b(r1.x, r1.y));
        const float s2 = wave_sum(g_a(r0.z) + g_a(r1.z)), s3 = wave_sum(g_b(r0.z, r0.w) + g_b(r1.z, r1.w));
        v2u o;
        o.x = pkbf(bflo(xv.x) + s0, bfhi(xv.x) + s1);
        o.y = pkbf(bflo(xv.y) + s2, bfhi(xv.y) + s3);
        if (lane == 0 && !dry) {
            *(GAS v2u*)(out + own * 4) = o;
            if (b == 0) __hip_atomic_store((GAS unsigned*)(ws + WS_EPOCH), tag, __ATOMIC_RELAXED, __HIP_MEMORY_SCOPE_AGENT);
        }
        STAMP(3);
        END_PASSES
        STAMP_FLUSH();
        return;
    }
    if (wave == 3) {
        PASSES(dry)
        const __amdgpu_buffer_rsrc_t wsr = __builtin_amdgcn_make_buffer_rsrc((void*)ws, 0, dry ? 0 : (int)WS_END, 0x00020000);
        wait_ge(FL + F_APUB, dry ? 0 : 4);
        const unsigned tag = FL[F_TAG], tagw = tag << 16;
        if (!dry) __builtin_amdgcn_s_sleep(S2_SLEEP);
        STAMP(1);
        v4u r[8], ml;
        bool ok[8], okm = false;
#pragma unroll
        for (int i = 0; i < 8; ++i) ok[i] = false;
#pragma nounroll
        for (unsigned sp = 0; sp < GLB_SPIN; ++sp) {
            if (!okm) ml = LD(((ks >> 2) * 64 + lane) * 16, WS_S2M);
#pragma unroll
            for (int i = 0; i < 8; ++i) if (!ok[i]) r[i] = LD(ks * 8192 + lane * 16, WS_S2O + i * 1024);
            okm = (ml.y == tag) & (ml.w == tag);
            bool all = okm;
#pragma unroll
            for (int i = 0; i < 8; ++i) { ok[i] = tag_ok(r[i], tagw); all &= ok[i]; }
            if (__all(all) || dry) break;
            __builtin_amdgcn_s_sleep(2);
            CBAR();
        }
        STAMP(2);
        const float M = wave_max(uf(ml.x));
        const float wgt = __builtin_amdgcn_exp2f(uf(ml.x) - M);
        const float rl = __builtin_amdgcn_rcpf(wave_sum(wgt * uf(ml.z)));
        f32x4 acc = {0.f, 0.f, 0.f, 0.f};
#pragma unroll
        for (int i = 0; i < 8; ++i) {
            const float w = uf(__builtin_amdgcn_ds_bpermute((8 * i + (lane >> 3)) * 4, fu(wgt)));
            acc[0] = fmaf(w, g_a(r[i].x), acc[0]); acc[1] = fmaf(w, g_b(r[i].x, r[i].y), acc[1]);
            acc[2] = fmaf(w, g_a(r[i].z), acc[2]); acc[3] = fmaf(w, g_b(r[i].z, r[i].w), acc[3]);
        }
#pragma unroll
        for (int k = 0; k < 4; ++k) acc[k] = xrow_sum(acc[k] + dppm<0x128>(acc[k])) * rl;
        *(LAS v2u*)(L + ((lane < 8 && !dry) ? L_O : L_JUNK) + lane * 8) = v2u{pkbf(acc[0], acc[1]), pkbf(acc[2], acc[3])};
        SIGNAL(F_O);
        STAMP(3);
        END_PASSES
        STAMP_FLUSH();
        return;
    }
    const int q4 = lj >> 2, p4 = lane & 3;
    const unsigned key = (unsigned)(q4 | ((lg & 1) << 2));
    if (wave < 8) {
        const int aw = wave - 4;
        PASSES(dry)
        const __amdgpu_buffer_rsrc_t wsr = __builtin_amdgcn_make_buffer_rsrc((void*)ws, 0, dry ? 0 : (int)WS_END, 0x00020000);
        wait_ge(FL + F_PROG, dry ? 0 : S1_AT);
        wait_ge(FL + F_XN, dry ? 0 : 1);
        const unsigned tag = FL[F_TAG], tagw = tag << 16;
        STAMP(1);
        {
            v4u r[4];
            bool ok[4] = {false, false, false, false};
#pragma nounroll
            for (unsigned sp = 0; sp < GLB_SPIN; ++sp) {
#pragma unroll
                for (int k = 0; k < 4; ++k) if (!ok[k]) r[k] = LD(gkv * 4096 + aw * 1024 + lane * 16, WS_S1 + k * 16384);
                bool all = true;
#pragma unroll
                for (int k = 0; k < 4; ++k) { ok[k] = tag_ok(r[k], tagw); all &= ok[k]; }
                if (__all(all) || dry) break;
                __builtin_amdgcn_s_sleep(2);
                CBAR();
            }
            const float q0 = (g_a(r[0].x) + g_a(r[1].x)) + (g_a(r[2].x) + g_a(r[3].x));
            const float q1 = (g_b(r[0].x, r[0].y) + g_b(r[1].x, r[1].y)) + (g_b(r[2].x, r[2].y) + g_b(r[3].x, r[3].y));
            const float q2 = (g_a(r[0].z) + g_a(r[1].z)) + (g_a(r[2].z) + g_a(r[3].z));
            const float q3 = (g_b(r[0].z, r[0].w) + g_b(r[1].z, r[1].w)) + (g_b(r[2].z, r[2].w) + g_b(r[3].z, r[3].w));
            *(LAS v2u*)(L + (dry ? L_JUNK : L_Q + 2 * aw * 272) + (lane >> 5) * 272 + (lane & 31) * 8) = v2u{pkbf(q0, q1), pkbf(q2, q3)};
            SIGNAL(F_QCNT);
        }
        STAMP(2);
        wait_ge(FL + F_PROG, dry ? 0 : 64);
        v4u kf[4];
#pragma unroll
        for (int s = 0; s < 4; ++s) kf[s] = *(LAS v4u*)(L + L_K + (16 * aw + lj) * 256 + (((4 * s + lg) ^ lj) << 4));
        v4u vf[2][2];
#pragma unroll
        for (int c = 0; c < 2; ++c)
#pragma unroll
            for (int s = 0; s < 2; ++s)
                vf[c][s] = trfrag(L, L_V + (32 * s + 4 * lg + q4) * 256 + (((2 * aw + c) ^ key) << 5) + p4 * 8, 16 * 256);
        wait_ge<false>(FL + F_QCNT, dry ? 0 : 4);
        STAMP(4);
        v4u qf[4];
#pragma unroll
        for (int s = 0; s < 4; ++s) qf[s] = *(LAS v4u*)(L + L_Q + (lj & 7) * 272 + s * 64 + lg * 16);
        __builtin_amdgcn_sched_barrier(0);
        f32x4 sc = {0.f, 0.f, 0.f, 0.f};
#pragma unroll
        for (int s = 0; s < 4; ++s) sc = mfma(kf[s], qf[s], sc);
#pragma unroll
        for (int r = 0; r < 4; ++r) sc[r] = rbf(rbf(sc[r]) * 0.08838834764831845f) * LOG2E;
        *(LAS f32x4*)(L + ((lj < 8 && !dry) ? L_SC + lj * 272 + aw * 64 + lg * 16 : L_JUNK + lane * 16)) = sc;
        SIGNAL(F_SCNT);
        wait_ge<false>(FL + F_SCNT, dry ? 0 : 4);
        STAMP(5);
        f32x4 sv[4];
#pragma unroll
        for (int t = 0; t < 4; ++t) sv[t] = *(LAS f32x4*)(L + L_SC + (lj & 7) * 272 + t * 64 + lg * 16);
        float m = sv[0][0];
#pragma unroll
        for (int t = 0; t < 4; ++t)
#pragma unroll
            for (int r = 0; r < 4; ++r) m = fmaxf(m, sv[t][r]);
        m = xrow_max(m);
        float l = 0.f;
#pragma unroll
        for (int t = 0; t < 4; ++t)
#pragma unroll
            for (int r = 0; r < 4; ++r) { sv[t][r] = __builtin_amdgcn_exp2f(sv[t][r] - m); l += sv[t][r]; }
        f32x4 o0 = {0.f, 0.f, 0.f, 0.f}, o1 = {0.f, 0.f, 0.f, 0.f};
#pragma unroll
        for (int s = 0; s < 2; ++s) {
            const v4u pf = {pkbf(sv[2 * s][0], sv[2 * s][1]), pkbf(sv[2 * s][2], sv[2 * s][3]), pkbf(sv[2 * s + 1][0], sv[2 * s + 1][1]), pkbf(sv[2 * s + 1][2], sv[2 * s + 1][3])};
            o0 = mfma(vf[0][s], pf, o0);
            o1 = mfma(vf[1][s], pf, o1);
        }
        l = xrow_sum(l);
        const int head = gkv * 8 + (lj & 7);
        if (aw == 0 && lane < 8) ST((v4u{fu(m), tag, fu(l), tag}), (head * 64 + split) * 16, WS_S2M);
        const f32x4 o = lj < 8 ? o0 : o1;
        ST(pack4(o[0], o[1], o[2], o[3], tagw), ((head * 4 + aw) * 64 + split) * 128 + (lj >> 3) * 64 + lg * 16, WS_S2O);
        SIGNAL(F_APUB);
        STAMP(3);
        END_PASSES
        STAMP_FLUSH();
        return;
    }
    const int gw = wave - 8;
    PASSES(dry)
    const __amdgpu_buffer_rsrc_t wsr = __builtin_amdgcn_make_buffer_rsrc((void*)ws, 0, dry ? 0 : (int)WS_END, 0x00020000);
    wait_ge(FL + F_XN, dry ? 0 : 1);
    const unsigned tagw = FL[F_TAG] << 16;
    if (gw < 4) {
        const unsigned row = 4 * lg + q4;
        const unsigned aoff = L_A + row * 128 + ((gw ^ ((row >> 1) & 3)) << 5) + p4 * 8;
        const unsigned xoff = L_XN + (b >> 6) * 512 + lg * 8;
        v4u xq[8];
#pragma unroll
        for (int s = 0; s < 8; ++s) xq[s] = xfrag(L, xoff + s * 64);
        wait_ge(FL + F_PROG, dry ? 0 : 32);
        STAMP(1);
        f32x4 acc = {0.f, 0.f, 0.f, 0.f};
        v4u af[8];
#pragma unroll
        for (int s = 0; s < 8; ++s) af[s] = trfrag(L, aoff + s * 32 * 128, 16 * 128);
        __builtin_amdgcn_sched_barrier(0);
#pragma unroll
        for (int s = 0; s < 8; ++s) acc = mfma(af[s], xq[s], acc);
        if (lj == 0) ST(pack4(acc[0], acc[1], acc[2], acc[3], tagw), ((b >> 6) * 4096 + (b & 63) * 64 + gw * 16 + lg * 4) * 4, WS_S1);
        SIGNAL(F_WQDONE);
        STAMP(2);
    }
    if (has1) {
        const int ct = b % 28, k8 = b / 28;
        const unsigned aoff = (4 * lg + q4) * 256 + ((gw ^ key) << 5) + p4 * 8;
        v4u xf[4];
#pragma unroll
        for (int s = 0; s < 4; ++s) xf[s] = xfrag(L, L_XN + k8 * 256 + lg * 8 + s * 64);
        f32x4 ga = {0.f, 0.f, 0.f, 0.f}, ua = {0.f, 0.f, 0.f, 0.f};
        wait_ge(FL + F_PROG, dry ? 0 : 96);
        v4u af[4];
#pragma unroll
        for (int s = 0; s < 4; ++s) af[s] = trfrag(L, L_C + aoff + s * 32 * 256, 16 * 256);
        SIGNAL(F_GDONE);
#pragma unroll
        for (int s = 0; s < 4; ++s) ga = mfma(af[s], xf[s], ga);
        wait_ge(FL + F_PROG, dry ? 0 : 128);
        STAMP(3);
#pragma unroll
        for (int s = 0; s < 4; ++s) af[s] = trfrag(L, L_D + aoff + s * 32 * 256, 16 * 256);
        __builtin_amdgcn_sched_barrier(0);
#pragma unroll
        for (int s = 0; s < 4; ++s) ua = mfma(af[s], xf[s], ua);
        if (lj < 2)
            ST(pack4(lj ? ga[2] : ga[0], lj ? ua[2] : ua[0], lj ? ga[3] : ga[1], lj ? ua[3] : ua[1], tagw),
               (k8 * FF + ct * 128 + gw * 16 + lg * 4 + lj * 2) * 8, WS_SH);
        STAMP(4);
    }
    {
        const unsigned rbase = (4 * lg + q4) * 1024 + p4 * 8;
        f32x4 c[4];
        v4u af[4];
        wait_ge(FL + F_PROG, dry ? 0 : pbase + 32);
#pragma unroll
        for (int t = 0; t < 4; ++t) af[t] = trfrag(L, L_A + rbase + (((4 * gw + t) ^ key) << 5), 16 * 1024);
        wait_ge(FL + F_O, dry ? 0 : 1);
        STAMP(5);
        const v4u of = xfrag(L, L_O + lg * 8);
#pragma unroll
        for (int t = 0; t < 4; ++t) c[t] = mfma(af[t], of, f32x4{0.f, 0.f, 0.f, 0.f});
        wait_ge(FL + F_H, dry ? 0 : 1);
        const v4u hf = xfrag(L, L_H + lg * 8);
        wait_ge<false>(FL + F_PROG, dry ? 0 : pbase + 60);
        STAMP(6);
#pragma unroll
        for (int t = 0; t < 4; ++t) {
            af[t] = trfrag(L, L_C + rbase + (((4 * gw + t) ^ key) << 5), 16 * 1024);
            if (lg == 3) { af[t].z = 0u; af[t].w = 0u; }
        }
#pragma unroll
        for (int t = 0; t < 4; ++t) c[t] = mfma(af[t], hf, c[t]);
        const f32x4 cs = lj == 0 ? c[0] : lj == 1 ? c[1] : lj == 2 ? c[2] : c[3];
        if (lj < 4) ST(pack4(cs[0], cs[1], cs[2], cs[3], tagw), (ks * 1024 + ct2 * 512 + gw * 64 + lj * 16 + lg * 4) * 4, WS_S3);
        SIGNAL(F_PUB);
        STAMP(7);
    }
    END_PASSES
    STAMP_FLUSH();
}

extern "C" void kernel_launch(void* const* d_in, const int* in_sizes, int n_in, void* d_out, int out_size, void* d_ws, size_t ws_size,
                              hipStream_t stream) {
    (void)in_sizes; (void)n_in; (void)out_size; (void)ws_size;
    (void)hipFuncSetAttribute((const void*)fused_decode, hipFuncAttributeMaxDynamicSharedMemorySize, (int)LDS_BYTES);
    fused_decode<<<256, 1024, LDS_BYTES, stream>>>((const u16*)d_in[0], (const u16*)d_in[1], (const u16*)d_in[2], (const u16*)d_in[3],
                                                   (const u16*)d_in[4], (const u16*)d_in[6], (const u16*)d_in[7], (const u16*)d_in[8],
                                                   (u16*)d_out, (unsigned char*)d_ws);
}
```

```cpp
#include <hip/hip_runtime.h>
#include <hip/hip_bf16.h>
#include <stdint.h>

#define LAS __attribute__((address_space(3)))
#define GAS __attribute__((address_space(1)))
typedef unsigned short u16;
typedef unsigned v4u __attribute__((ext_vector_type(4)));
typedef unsigned v2u __attribute__((ext_vector_type(2)));
typedef int v4i __attribute__((ext_vector_type(4)));
typedef float f32x4 __attribute__((ext_vector_type(4)));
typedef float f32x2 __attribute__((ext_vector_type(2)));
typedef short s16x4 __attribute__((ext_vector_type(4)));
typedef __bf16 bf16x8 __attribute__((ext_vector_type(8)));
typedef __bf16 bf16x2 __attribute__((ext_vector_type(2)));

constexpr int DM = 1024, SK = 32768, WIN = 4096, FF = 3584;
constexpr unsigned WS_EPOCH = 0;
constexpr unsigned WS_S1 = 4096;
constexpr unsigned WS_SH = WS_S1 + 4 * 4096 * 4;
constexpr unsigned WS_S2O = WS_SH + 16 * 3584 * 8;
constexpr unsigned WS_S2M = WS_S2O + 64 * 32 * 256;
constexpr unsigned WS_S3 = WS_S2M + 32 * 32 * 16;
constexpr unsigned WS_END = WS_S3 + 128 * 1024 * 4;
constexpr unsigned L_X = 0;
constexpr unsigned L_Y = 65536;
constexpr unsigned L_XN = 131072;
constexpr unsigned L_Q = L_XN + 2048;
constexpr unsigned L_P = L_Q + 2304;
constexpr unsigned L_ML = L_P + 4096;
constexpr unsigned L_O = L_ML + 1024;
constexpr unsigned L_FLAG = L_O + 128;
constexpr unsigned L_JUNK = L_FLAG + 64;
constexpr unsigned L_TS = L_JUNK + 1024;
constexpr unsigned LDS_BYTES = L_TS + 1024;
enum { F_PROG = 0, F_XN = 1, F_TAG = 2, F_WQDONE = 3, F_GDONE = 4 };

#define D_A 28
#define D_W 16
#define W_PRE 2
#define W_DELAY 0
#define S3_SLEEP 16
#define D_B 32
#define D_D 28
#define DN_GATE 0
#define DN_PRE 0
#define DN_DELAY 4
#define WS_OFF 0
#define S1_AT (96 - D_A)
#define SH_AT 24
#define S2_SLEEP 48
constexpr unsigned LDS_SPIN = 1u << 20, GLB_SPIN = 1u << 16;
constexpr int AUX_SC1 = 16;
constexpr float LOG2E = 1.4426950408889634f;

__device__ __forceinline__ float bflo(unsigned u) { return __builtin_bit_cast(float, u << 16); }
__device__ __forceinline__ float bfhi(unsigned u) { return __builtin_bit_cast(float, u & 0xffff0000u); }
__device__ __forceinline__ float rbf(float f) { return (float)(__bf16)f; }
__device__ __forceinline__ unsigned pkbf(float lo, float hi) { bf16x2 v = {(__bf16)lo, (__bf16)hi}; return __builtin_bit_cast(unsigned, v); }
__device__ __forceinline__ unsigned fu(float f) { return __builtin_bit_cast(unsigned, f); }
__device__ __forceinline__ float uf(unsigned u) { return __builtin_bit_cast(float, u); }
__device__ __forceinline__ float fmx(float a, float b) { return __builtin_elementwise_maximum(a, b); }
__device__ __forceinline__ unsigned g_lo(float a, float b) { return __builtin_amdgcn_perm(fu(b), fu(a), 0x05030201u); }
__device__ __forceinline__ unsigned g_hi(float b, unsigned tagw) { return __builtin_amdgcn_perm(tagw, fu(b), 0x07060302u); }
__device__ __forceinline__ v4u pack4(float a, float b, float c, float d, unsigned tagw) { return v4u{g_lo(a, b), g_hi(b, tagw), g_lo(c, d), g_hi(d, tagw)}; }
__device__ __forceinline__ float g_a(unsigned lo) { return uf(lo << 8); }
__device__ __forceinline__ float g_b(unsigned lo, unsigned hi) { return uf(__builtin_amdgcn_perm(hi, lo, 0x0504030cu)); }
__device__ __forceinline__ bool tag_ok(v4u r, unsigned tagw) { return (((r.y ^ tagw) | (r.w ^ tagw)) >> 16) == 0u; }

template <int CTRL> __device__ __forceinline__ float dppm(float v) {
    return __builtin_bit_cast(float, __builtin_amdgcn_mov_dpp(__builtin_bit_cast(int, v), CTRL, 0xf, 0xf, true));
}
__device__ __forceinline__ float row_sum(float v) {
    v += dppm<0xB1>(v); v += dppm<0x4E>(v); v += dppm<0x141>(v); v += dppm<0x128>(v);
    return v;
}
__device__ __forceinline__ float half_sum(float x) {
    auto t = __builtin_amdgcn_permlane32_swap(fu(x), fu(x), false, false);
    return uf(t[0]) + uf(t[1]);
}
__device__ __forceinline__ float xrow_max(float x) {
    auto s = __builtin_amdgcn_permlane16_swap(fu(x), fu(x), false, false);
    x = fmx(uf(s[0]), uf(s[1]));
    auto t = __builtin_amdgcn_permlane32_swap(fu(x), fu(x), false, false);
    return fmx(uf(t[0]), uf(t[1]));
}
__device__ __forceinline__ float xrow_sum(float x) {
    auto s = __builtin_amdgcn_permlane16_swap(fu(x), fu(x), false, false);
    return half_sum(uf(s[0]) + uf(s[1]));
}
__device__ __forceinline__ float wave_sum(float v) { return xrow_sum(row_sum(v)); }
__device__ __forceinline__ float wave_max(float x) {
    x = fmx(x, dppm<0xB1>(x)); x = fmx(x, dppm<0x4E>(x)); x = fmx(x, dppm<0x141>(x)); x = fmx(x, dppm<0x128>(x));
    return xrow_max(x);
}
__device__ __forceinline__ f32x4 mfma(v4u a, v4u b, f32x4 c) {
    return __builtin_amdgcn_mfma_f32_16x16x32_bf16(__builtin_bit_cast(bf16x8, a), __builtin_bit_cast(bf16x8, b), c, 0, 0, 0);
}
__device__ __forceinline__ f32x4 mfma16(v2u a, v2u b, f32x4 c) {
    return __builtin_amdgcn_mfma_f32_16x16x16bf16_1k(__builtin_bit_cast(s16x4, a), __builtin_bit_cast(s16x4, b), c, 0, 0, 0);
}
__device__ __forceinline__ v2u trread(LAS unsigned char* L, unsigned off) {
    return __builtin_bit_cast(v2u, __builtin_amdgcn_ds_read_tr16_b64_v4i16((LAS s16x4*)(L + off)));
}
__device__ __forceinline__ v4u trfrag(LAS unsigned char* L, unsigned off, unsigned toff) {
    const v2u a = trread(L, off), b = trread(L, off + toff);
    return v4u{a.x, a.y, b.x, b.y};
}
__device__ __forceinline__ v4u xfrag(LAS unsigned char* L, unsigned off) {
    const v2u a = *(LAS v2u*)(L + off), b = *(LAS v2u*)(L + off + 32);
    return v4u{a.x, a.y, b.x, b.y};
}
#define CBAR() asm volatile("" ::: "memory")
#define LGKM0() asm volatile("s_waitcnt lgkmcnt(0)" ::: "memory")
#define BARRIER() asm volatile("s_waitcnt lgkmcnt(0)\n\ts_barrier" ::: "memory")
#define PIN(v) asm volatile("" : "+v"(v))
__device__ __forceinline__ void wait_ge(volatile LAS unsigned* w, unsigned want) {
#pragma nounroll
    for (unsigned s = 0; s < LDS_SPIN; ++s) {
        if ((unsigned)__builtin_amdgcn_readfirstlane(*w) >= want) break;
        __builtin_amdgcn_s_sleep(1);
    }
    CBAR();
}
#define SIGNAL(flag) asm volatile("s_waitcnt lgkmcnt(0)\n\tds_add_u32 %0, %1 offset:%2" ::"v"(lane == 0 ? L_FLAG : L_JUNK + 4u * lane), "v"(1u), "i"(4 * (flag)) : "memory")
__device__ __forceinline__ v4i mkrsrc(const void* p, unsigned bytes) {
    const uint64_t a = (uint64_t)p;
    return v4i{(int)(unsigned)a, (int)((unsigned)(a >> 32) & 0xffffu), (int)bytes, 0x00020000};
}
#define VMWAIT(n) asm volatile("s_waitcnt vmcnt(%0)" ::"i"(n) : "memory")
#define LD(voff, soff) __builtin_amdgcn_raw_buffer_load_b128(wsr, (voff), (soff), AUX_SC1)
#define ST(v, voff) __builtin_amdgcn_raw_buffer_store_b128((v), wsr, (voff), 0, AUX_SC1)

#define STAMP(i) do { } while (0)
#define STAMP_FLUSH() do { } while (0)
#define STAMP_OUT() do { } while (0)

template <int DEP>
__device__ __forceinline__ void dma4(v4i rs, v4u v, unsigned& soff, unsigned sstep, unsigned lds) {
    asm volatile("s_mov_b32 m0, %[lds]\n\t"
                 "s_nop 0\n\t"
                 "buffer_load_dwordx4 %[v0], %[rs], %[so] offen nt lds\n\t"
                 "s_add_u32 m0, m0, 0x400\n\t"
                 "s_add_u32 %[so], %[so], %[st]\n\t"
                 "buffer_load_dwordx4 %[v1], %[rs], %[so] offen nt lds\n\t"
                 "s_add_u32 m0, m0, 0x400\n\t"
                 "s_add_u32 %[so], %[so], %[st]\n\t"
                 "s_waitcnt vmcnt(%[dep])\n\t"
                 "buffer_load_dwordx4 %[v2], %[rs], %[so] offen nt lds\n\t"
                 "s_add_u32 m0, m0, 0x400\n\t"
                 "s_add_u32 %[so], %[so], %[st]\n\t"
                 "buffer_load_dwordx4 %[v3], %[rs], %[so] offen nt lds\n\t"
                 "s_add_u32 %[so], %[so], %[st]\n\t"
                 "s_waitcnt vmcnt(%[dep])"
                 : [so] "+s"(soff)
                 : [lds] "s"(lds), [v0] "v"(v.x), [v1] "v"(v.y), [v2] "v"(v.z), [v3] "v"(v.w), [rs] "s"(rs), [st] "s"(sstep), [dep] "i"(DEP)
                 : "memory", "scc");
}
template <int DEP>
__device__ __forceinline__ void stream(v4i rs, v4u va, v4u vb, unsigned soff, unsigned sstep, unsigned lds, int ng, int& prog, unsigned progaddr) {
    const unsigned end = lds + ng * 4096;
#pragma nounroll
    do {
        dma4<DEP>(rs, va, soff, sstep, lds);
        prog += 4;
        asm volatile("ds_max_i32 %0, %1" ::"v"(progaddr), "v"(prog - DEP) : "memory");
        dma4<DEP>(rs, vb, soff, sstep, lds + 4096);
        prog += 4; lds += 8192;
        asm volatile("ds_max_i32 %0, %1" ::"v"(progaddr), "v"(prog - DEP) : "memory");
    } while (lds != end);
}

__global__ void __launch_bounds__(1024, 4)
fused_decode(const u16* __restrict__ wq, const u16* __restrict__ x, unsigned char* ws, const u16* __restrict__ kc, const u16* __restrict__ vc,
             const u16* __restrict__ wo, const u16* __restrict__ wg, const u16* __restrict__ wu, const u16* __restrict__ wd, u16* __restrict__ out) {
    extern __shared__ __attribute__((aligned(16))) unsigned char smem[];
    LAS unsigned char* L = (LAS unsigned char*)smem;
    volatile LAS unsigned* FL = (volatile LAS unsigned*)(L + L_FLAG);
    const int tid = threadIdx.x, lane = tid & 63, wave = __builtin_amdgcn_readfirstlane(tid >> 6), b = blockIdx.x;
    STAMP(0);
    FL[lane & 15] = 0u;
    v4u xa, xb;
    unsigned ep;
    {
        const int rl = lane >> 3, p = lane & 7;
        const unsigned v0 = rl * 8192 + (((p >> 1) ^ ((rl >> 1) & 3)) << 5) + (p & 1) * 16;
        unsigned so = (unsigned)(b >> 6) * 256 * 8192 + (b & 63) * 128;
        const v4i rs = mkrsrc(wq, DM * 4096 * 2);
        const unsigned l16 = lane * 16, zero = 0;
        uint64_t pc;
        unsigned coff = (b >> 3) * 1024 + l16;
        asm volatile("s_cmp_lg_u32 %[wave], 0\n\t"
                     "s_cbranch_scc1 1f\n\t"
                     "s_mov_b32 m0, %[lds]\n"
                     "0:\n\t"
                     "s_nop 0\n\t"
                     "buffer_load_dwordx4 %[v0], %[rs], %[so] offen nt lds\n\t"
                     "s_add_u32 m0, m0, 0x400\n\t"
                     "s_add_u32 %[so], %[so], 0x10000\n\t"
                     "buffer_load_dwordx4 %[v0], %[rs], %[so] offen nt lds\n\t"
                     "s_add_u32 m0, m0, 0x400\n\t"
                     "s_add_u32 %[so], %[so], 0x10000\n\t"
                     "buffer_load_dwordx4 %[v0], %[rs], %[so] offen nt lds\n\t"
                     "s_add_u32 m0, m0, 0x400\n\t"
                     "s_add_u32 %[so], %[so], 0x10000\n\t"
                     "buffer_load_dwordx4 %[v0], %[rs], %[so] offen nt lds\n\t"
                     "s_add_u32 m0, m0, 0x400\n\t"
                     "s_add_u32 %[so], %[so], 0x10000\n\t"
                     "s_cmp_lg_u32 m0, %[lend]\n\t"
                     "s_cbranch_scc1 0b\n\t"
                     "s_branch 3f\n"
                     "1:\n\t"
                     "s_cmp_lg_u32 %[wave], 1\n\t"
                     "s_cbranch_scc1 2f\n\t"
                     "global_load_dwordx4 %[xa], %[l16], %[x]\n\t"
                     "global_load_dwordx4 %[xb], %[l16], %[x] offset:1024\n\t"
                     "global_load_dword %[ep], %[zero], %[ws] sc1\n\t"
                     "s_branch 3f\n"
                     "2:\n\t"
                     "s_cmp_lg_u32 %[wave], 2\n\t"
                     "s_cbranch_scc1 3f\n\t"
                     "s_mov_b32 m0, %[junk]\n\t"
                     ".subsection 1\n"
                     "5:\n\t"
                     ".subsection 0\n\t"
                     "v_min_u32 %[coff], 5b-4f-16, %[coff]\n\t"
                     "s_getpc_b64 %[pc]\n"
                     "4:\n\t"
                     "global_load_lds_dwordx4 %[coff], %[pc]\n"
                     "3:\n\t"
                     "s_waitcnt lgkmcnt(0)\n\t"
                     "s_barrier"
                     : [so] "+s"(so), [xa] "=&v"(xa), [xb] "=&v"(xb), [ep] "=&v"(ep), [pc] "=&s"(pc), [coff] "+v"(coff)
                     : [wave] "s"(wave), [lds] "i"(L_Y), [lend] "i"(L_Y + 24576), [junk] "i"(L_JUNK), [v0] "v"(v0), [rs] "s"(rs), [l16] "v"(l16), [zero] "v"(zero), [x] "s"(x), [ws] "s"(ws)
                     : "memory", "scc");
        if (wave == 0) {
            const int xc = b & 7, bi = b >> 3, idx = xc * 16 + (bi >> 1), ks = idx >> 1, ct2 = idx & 1;
            const int lj = lane & 15, lg = lane >> 4;
            const unsigned pa = lane == 0 ? L_FLAG + 4 * F_PROG : L_JUNK + 4 * lane;
            int cnt = 24;
            PIN(cnt);
            stream<D_A>(rs, v4u{v0, v0, v0, v0}, v4u{v0, v0, v0, v0}, so, 65536, L_Y + 24576, 2, cnt, pa);
            STAMP(1);
            v4u va, vb;
#pragma unroll
            for (int i = 0; i < 4; ++i) { va[i] = l16 ^ (i << 5); vb[i] = l16 ^ ((i + 4) << 5); }
            if (bi & 1) {
                const size_t kvoff = ((size_t)(xc >> 1) * SK + (SK - WIN) + (idx & 31) * 128) * 128;
                const unsigned k0 = lg * 256 + ((lj ^ lg) << 4);
                const v4u kv = {k0, k0 ^ 0x40u, k0 ^ 0x80u, k0 ^ 0xC0u};
                stream<D_A>(mkrsrc(kc + kvoff, 32768), kv, kv, 0, 1024, L_X, 8, cnt, pa);
                const unsigned u0 = lg * 256 + (((lj >> 1) ^ lg) << 5) + (lj & 1) * 16;
                const v4u vv = {u0, u0 ^ 0x80u, u0, u0 ^ 0x80u};
                stream<D_A>(mkrsrc(vc + kvoff, 32768), vv, vv, 0, 1024, L_X + 32768, 8, cnt, pa);
                STAMP(2);
                VMWAIT(0);
                STAMP(3);
                BARRIER();
                const v4i wr = mkrsrc(wo, 4096 * DM * 2);
                const unsigned so2 = (unsigned)ks * 64 * 2048 + ct2 * 1024;
                if (W_PRE) stream<D_W>(wr, va, vb, so2, 2048, L_Y, W_PRE, cnt, pa);
                BARRIER();
                if (W_DELAY) __builtin_amdgcn_s_sleep(W_DELAY);
                stream<D_W>(wr, va, vb, so2 + W_PRE * 4 * 2048, 2048, L_Y + W_PRE * 4096, 16 - W_PRE, cnt, pa);
            } else {
                v4u ga, gb;
#pragma unroll
                for (int i = 0; i < 4; ++i) { ga[i] = va[i] < 896 ? va[i] : 0x40000000u; gb[i] = vb[i] < 896 ? vb[i] : 0x40000000u; }
                const unsigned so2 = (unsigned)(idx >> 3) * 64 * 7168 + (idx & 7) * 896;
                stream<D_B>(mkrsrc(wg, DM * FF * 2), ga, gb, so2, 7168, L_X, 16, cnt, pa);
                STAMP(2);
                wait_ge(FL + F_WQDONE, 4);
                stream<D_B>(mkrsrc(wu, DM * FF * 2), ga, gb, so2, 7168, L_Y, 16, cnt, pa);
                STAMP(3);
                wait_ge(FL + F_GDONE, 14);
                const v4i dr = mkrsrc(wd, FF * DM * 2);
                const unsigned so3 = (unsigned)ks * 56 * 2048 + ct2 * 1024;
                stream<D_D>(dr, va, vb, so3, 2048, L_X, 14, cnt, pa);
                VMWAIT(24);
                asm volatile("ds_max_i32 %0, %1" ::"v"(pa), "v"(cnt - 24) : "memory");
            }
            STAMP(4);
            VMWAIT(0);
            STAMP(5);
            BARRIER();
            STAMP_FLUSH();
            return;
        }
    }
    const int xc = b & 7, bi = b >> 3;
    const bool att = bi & 1;
    const int idx = xc * 16 + (bi >> 1);
    const int ks = idx >> 1, ct2 = idx & 1;
    const int lj = lane & 15, lg = lane >> 4;
    const int q4 = lj >> 2, p4 = lane & 3;
    const unsigned key = (unsigned)(q4 | ((lg & 1) << 2));
    const __amdgpu_buffer_rsrc_t wsr = __builtin_amdgcn_make_buffer_rsrc((void*)ws, 0, (int)WS_END, 0x00020000);

    if (wave == 1) {
        asm volatile("s_waitcnt vmcnt(0)" : "+v"(xa), "+v"(xb), "+v"(ep)::"memory");
        float v[16];
#pragma unroll
        for (int i = 0; i < 4; ++i) { v[2 * i] = bflo(xa[i]); v[2 * i + 1] = bfhi(xa[i]); v[8 + 2 * i] = bflo(xb[i]); v[9 + 2 * i] = bfhi(xb[i]); }
        float s = 0.f;
#pragma unroll
        for (int i = 0; i < 16; ++i) s += v[i];
        const float mean = wave_sum(s) * (1.0f / DM);
        float q = 0.f;
#pragma unroll
        for (int i = 0; i < 16; ++i) { const float d = v[i] - mean; q = fmaf(d, d, q); }
        const float var = wave_sum(q) * (1.0f / DM);
        const float mb = rbf(mean), den = rbf(__builtin_amdgcn_sqrtf(rbf(rbf(var) + 1e-5f)));
        const float rden = __builtin_amdgcn_rcpf(den);
        v4u oa, ob;
#pragma unroll
        for (int i = 0; i < 4; ++i) {
            const unsigned da = pkbf(v[2 * i] - mb, v[2 * i + 1] - mb), db = pkbf(v[8 + 2 * i] - mb, v[9 + 2 * i] - mb);
            oa[i] = pkbf(bflo(da) * rden, bfhi(da) * rden);
            ob[i] = pkbf(bflo(db) * rden, bfhi(db) * rden);
        }
        *(LAS v4u*)(L + L_XN + lane * 16) = oa;
        *(LAS v4u*)(L + L_XN + 1024 + lane * 16) = ob;
        const unsigned tag = ep + 1u, tagw = tag << 16;
        if (lane == 0) FL[F_TAG] = tag;
        SIGNAL(F_XN);
        STAMP(1);
        const int own = xc * 32 + bi, col = own * 4 + ((lg & 1) << 1) + (lg >> 1);
        const float xres = bflo(x[col]);
        if (att) {
            BARRIER();
            BARRIER();
            __builtin_amdgcn_s_sleep(S2_SLEEP);
            STAMP(2);
            v4u r[8], mlv;
            bool ok[8], okm = false;
#pragma unroll
            for (int i = 0; i < 8; ++i) ok[i] = false;
#pragma nounroll
            for (unsigned sp = 0; sp < GLB_SPIN; ++sp) {
                if (!okm) mlv = LD(((ks >> 1) * 32 + (lane & 31)) * 16, WS_S2M);
#pragma unroll
                for (int i = 0; i < 8; ++i) if (!ok[i]) r[i] = LD(ks * 8192 + lane * 16, WS_S2O + i * 1024);
                okm = (mlv.y == tag) & (mlv.w == tag);
                bool all = okm;
#pragma unroll
                for (int i = 0; i < 8; ++i) { ok[i] = tag_ok(r[i], tagw); all &= ok[i]; }
                if (__all(all)) break;
                CBAR();
            }
            STAMP(3);
            const float Mx = wave_max(uf(mlv.x));
            const float wgt = __builtin_amdgcn_exp2f(uf(mlv.x) - Mx);
            const float rl = 2.0f * __builtin_amdgcn_rcpf(wave_sum(wgt * uf(mlv.z)));
            f32x4 acc = {0.f, 0.f, 0.f, 0.f};
#pragma unroll
            for (int i = 0; i < 8; ++i) {
                const float w = uf(__builtin_amdgcn_ds_bpermute((4 * i + lg) * 4, fu(wgt)));
                acc[0] = fmaf(w, g_a(r[i].x), acc[0]); acc[1] = fmaf(w, g_b(r[i].x, r[i].y), acc[1]);
                acc[2] = fmaf(w, g_a(r[i].z), acc[2]); acc[3] = fmaf(w, g_b(r[i].z, r[i].w), acc[3]);
            }
#pragma unroll
            for (int k = 0; k < 4; ++k) acc[k] = xrow_sum(acc[k]) * rl;
            if (lane < 16) *(LAS v2u*)(L + L_O + lane * 8) = v2u{pkbf(acc[0], acc[1]), pkbf(acc[2], acc[3])};
        } else {
            wait_ge(FL + F_PROG, 160 + SH_AT);
            STAMP(2);
            const int un = lane & 31;
            const unsigned off = ((lane >> 5) * FF + ks * 56 + (un < 28 ? un : 27) * 2) * 8;
            v4u r[8];
            bool ok[8];
#pragma unroll
            for (int i = 0; i < 8; ++i) ok[i] = false;
#pragma nounroll
            for (unsigned sp = 0; sp < GLB_SPIN; ++sp) {
#pragma unroll
                for (int i = 0; i < 8; ++i) if (!ok[i]) r[i] = LD(off, WS_SH + i * 2 * FF * 8);
                bool all = true;
#pragma unroll
                for (int i = 0; i < 8; ++i) { ok[i] = tag_ok(r[i], tagw); all &= ok[i]; }
                if (__all(all)) break;
                CBAR();
            }
            STAMP(3);
            float hv[2];
#pragma unroll
            for (int c = 0; c < 2; ++c) {
                float g = 0.f, u = 0.f;
#pragma unroll
                for (int i = 0; i < 8; ++i) { g += g_a(r[i][2 * c]); u += g_b(r[i][2 * c], r[i][2 * c + 1]); }
                g = rbf(half_sum(g)); u = rbf(half_sum(u));
                hv[c] = rbf(g * __builtin_amdgcn_rcpf(1.0f + __builtin_amdgcn_exp2f(-g * LOG2E))) * u;
            }
            if (lane < 32) *(LAS unsigned*)(L + L_O + lane * 4) = lane < 28 ? pkbf(hv[0], hv[1]) : 0u;
        }
        STAMP(4);
        BARRIER();
        __builtin_amdgcn_s_sleep(S3_SLEEP);
        v4u r0, r1;
        bool ok0 = false, ok1 = false;
#pragma nounroll
        for (unsigned sp = 0; sp < GLB_SPIN; ++sp) {
            if (!ok0) r0 = LD(lane * 4096 + own * 16, WS_S3);
            if (!ok1) r1 = LD(lane * 4096 + own * 16, WS_S3 + 64 * 4096);
            ok0 = tag_ok(r0, tagw); ok1 = tag_ok(r1, tagw);
            if (__all(ok0 & ok1)) break;
            CBAR();
        }
        STAMP(5);
        const float s0 = g_a(r0.x) + g_a(r1.x), s1 = g_b(r0.x, r0.y) + g_b(r1.x, r1.y);
        const float s2 = g_a(r0.z) + g_a(r1.z), s3 = g_b(r0.z, r0.w) + g_b(r1.z, r1.w);
        auto t01 = __builtin_amdgcn_permlane32_swap(fu(s0), fu(s1), false, false);
        auto t23 = __builtin_amdgcn_permlane32_swap(fu(s2), fu(s3), false, false);
        auto t = __builtin_amdgcn_permlane16_swap(fu(uf(t01[0]) + uf(t01[1])), fu(uf(t23[0]) + uf(t23[1])), false, false);
        const float tot = row_sum(uf(t[0]) + uf(t[1])) + xres;
        if (lj == 0) {
            *(GAS __bf16*)(out + col) = (__bf16)tot;
            if (b == 0 && lane == 0) __hip_atomic_store((GAS unsigned*)(ws + WS_EPOCH), tag, __ATOMIC_RELAXED, __HIP_MEMORY_SCOPE_AGENT);
        }
        STAMP(6);
        STAMP_FLUSH();
        STAMP_OUT();
        return;
    }
    if (att && wave < 4) { STAMP_FLUSH(); return; }
    wait_ge(FL + F_XN, 1);
    const unsigned tag = FL[F_TAG], tagw = tag << 16;
    if (wave >= 4 && wave < 8) {
        const int c = wave - 4;
        const unsigned row = 4 * lg + q4;
        const unsigned aoff = L_Y + row * 128 + ((c ^ ((row >> 1) & 3)) << 5) + p4 * 8;
        const unsigned xoff = L_XN + (b >> 6) * 512 + lg * 8;
        unsigned dst = ((b >> 6) * 4096 + (b & 63) * 64 + c * 16 + lg * 4) * 4 + WS_S1;
        PIN(dst);
        f32x4 acc = {0.f, 0.f, 0.f, 0.f};
#pragma nounroll
        for (int h = 0; h < 2; ++h) {
            v4u xq[4], af[4];
#pragma unroll
            for (int s = 0; s < 4; ++s) xq[s] = xfrag(L, xoff + h * 256 + s * 64);
            wait_ge(FL + F_PROG, 16 * h + 16);
#pragma unroll
            for (int s = 0; s < 4; ++s) af[s] = trfrag(L, aoff + h * 16384 + s * 4096, 2048);
            __builtin_amdgcn_sched_barrier(0);
#pragma unroll
            for (int s = 0; s < 4; ++s) acc = mfma(af[s], xq[s], acc);
        }
        if (lj == 0) ST(pack4(acc[0], acc[1], acc[2], acc[3], tagw), dst);
        SIGNAL(F_WQDONE);
        STAMP(1);
        if (att) { STAMP_FLUSH(); return; }
    }
    const unsigned rbase = (4 * lg + q4) * 1024 + p4 * 8;
    if (att) {
        const int aw = wave - 8, gkv = xc >> 1, split = idx & 31;
        unsigned a_q = L_Q + (lj & 7) * 272 + lg * 16, a_k = L_X + (16 * aw + lj) * 256, a_v = L_X + 32768 + (4 * lg + q4) * 256 + ((aw ^ key) << 5) + p4 * 8;
        unsigned a_p = L_P + lane * 8, a_ml = L_ML + (lj & 7) * 80;
        unsigned dst = (((gkv * 8 + lj) * 2 + (aw >> 2)) * 32 + split) * 256 + (aw & 3) * 64 + lg * 16 + WS_S2O;
        unsigned k0 = a_k + ((lg ^ lj) << 4), k1 = a_k + (((4 + lg) ^ lj) << 4), k2 = a_k + (((8 + lg) ^ lj) << 4), k3 = a_k + (((12 + lg) ^ lj) << 4);
        PIN(a_q); PIN(a_v); PIN(a_p); PIN(a_ml); PIN(dst);
        wait_ge(FL + F_PROG, S1_AT);
        STAMP(1);
        v4u kf[4];
        kf[0] = *(LAS v4u*)(L + k0); kf[1] = *(LAS v4u*)(L + k1); kf[2] = *(LAS v4u*)(L + k2); kf[3] = *(LAS v4u*)(L + k3);
        {
            v4u r0, r1;
#pragma nounroll
            for (unsigned sp = 0; sp < GLB_SPIN; ++sp) {
                r0 = LD((lane >> 5) * 16384 + gkv * 4096 + aw * 512 + (lane & 31) * 16, WS_S1);
                r1 = LD((lane >> 5) * 16384 + gkv * 4096 + aw * 512 + (lane & 31) * 16, WS_S1 + 32768);
                if (__all(tag_ok(r0, tagw) & tag_ok(r1, tagw))) break;
                CBAR();
            }
            STAMP(2);
            const float q0 = half_sum(g_a(r0.x) + g_a(r1.x)), q1 = half_sum(g_b(r0.x, r0.y) + g_b(r1.x, r1.y));
            const float q2 = half_sum(g_a(r0.z) + g_a(r1.z)), q3 = half_sum(g_b(r0.z, r0.w) + g_b(r1.z, r1.w));
            if (lane < 32) *(LAS v2u*)(L + L_Q + aw * 272 + lane * 8) = v2u{pkbf(q0, q1), pkbf(q2, q3)};
        }
        BARRIER();
        STAMP(3);
        v4u qf[4];
#pragma unroll
        for (int s = 0; s < 4; ++s) qf[s] = *(LAS v4u*)(L + a_q + s * 64);
        __builtin_amdgcn_sched_barrier(0);
        f32x4 sc = {0.f, 0.f, 0.f, 0.f};
#pragma unroll
        for (int s = 0; s < 4; ++s) sc = mfma(kf[s], qf[s], sc);
        __builtin_amdgcn_sched_barrier(0);
        v2u vf[8];
#pragma unroll
        for (int t = 0; t < 8; ++t) vf[t] = trread(L, a_v + t * 4096);
        __builtin_amdgcn_sched_barrier(0);
        {
            const unsigned r01 = pkbf(sc[0], sc[1]), r23 = pkbf(sc[2], sc[3]);
            const float SC = 0.08838834764831845f;
            const unsigned s01 = pkbf(bflo(r01) * SC, bfhi(r01) * SC), s23 = pkbf(bflo(r23) * SC, bfhi(r23) * SC);
            sc[0] = bflo(s01); sc[1] = bfhi(s01); sc[2] = bflo(s23); sc[3] = bfhi(s23);
        }
        const float m = xrow_max(fmx(fmx(sc[0], sc[1]), fmx(sc[2], sc[3]))) * LOG2E;
#pragma unroll
        for (int r = 0; r < 4; ++r) sc[r] = __builtin_amdgcn_exp2f(fmaf(sc[r], LOG2E, -m));
        *(LAS v2u*)(L + a_p + aw * 512) = v2u{pkbf(sc[0], sc[1]), pkbf(sc[2], sc[3])};
        const float l = xrow_sum((sc[0] + sc[1]) + (sc[2] + sc[3]));
        if (lane < 8) *(LAS v2u*)(L + a_ml + aw * 8) = v2u{fu(m), fu(l)};
        BARRIER();
        STAMP(4);
        v2u pf[8];
        f32x4 ml[4];
#pragma unroll
        for (int t = 0; t < 4; ++t) ml[t] = *(LAS f32x4*)(L + a_ml + t * 16);
#pragma unroll
        for (int t = 0; t < 8; ++t) pf[t] = *(LAS v2u*)(L + a_p + t * 512);
        __builtin_amdgcn_sched_barrier(0);
        f32x4 ot[8];
#pragma unroll
        for (int t = 0; t < 8; ++t) ot[t] = mfma16(vf[t], pf[t], f32x4{0.f, 0.f, 0.f, 0.f});
        const float M = fmx(fmx(fmx(ml[0][0], ml[0][2]), fmx(ml[1][0], ml[1][2])), fmx(fmx(ml[2][0], ml[2][2]), fmx(ml[3][0], ml[3][2])));
        f32x4 o = {0.f, 0.f, 0.f, 0.f};
        float lt = 0.f;
#pragma unroll
        for (int t = 0; t < 8; ++t) {
            const float w = __builtin_amdgcn_exp2f(ml[t >> 1][(t & 1) * 2] - M);
            lt = fmaf(w, ml[t >> 1][(t & 1) * 2 + 1], lt);
            o += w * ot[t];
        }
        if (lj < 8) ST(pack4(o[0], o[1], o[2], o[3], tagw), dst);
        if (aw == 0 && lane < 8) ST((v4u{fu(M), tag, fu(lt), tag}), ((gkv * 8 + lj) * 32 + split) * 16 + WS_S2M);
        STAMP(5);
    } else {
        const int gw = wave - 2;
        unsigned a0 = rbase + (((2 * gw) ^ key) << 5), a1 = rbase + (((2 * gw + 1) ^ key) << 5);
        unsigned dst = ((idx >> 3) * FF + (idx & 7) * 448 + gw * 32 + (lj >> 1) * 16 + lg * 4 + (lj & 1) * 2) * 8 + WS_SH;
        PIN(dst);
        v4u xf[2], af[2][2];
#pragma unroll
        for (int s = 0; s < 2; ++s) xf[s] = xfrag(L, L_XN + (idx >> 3) * 128 + lg * 8 + s * 64);
        wait_ge(FL + F_PROG, 96);
#pragma unroll
        for (int s = 0; s < 2; ++s) { af[s][0] = trfrag(L, L_X + a0 + s * 32768, 16384); af[s][1] = trfrag(L, L_X + a1 + s * 32768, 16384); }
        SIGNAL(F_GDONE);
        f32x4 g0 = mfma(af[0][0], xf[0], f32x4{0.f, 0.f, 0.f, 0.f}), g1 = mfma(af[0][1], xf[0], f32x4{0.f, 0.f, 0.f, 0.f});
        g0 = mfma(af[1][0], xf[1], g0); g1 = mfma(af[1][1], xf[1], g1);
        wait_ge(FL + F_PROG, 128);
        f32x4 u0 = mfma(trfrag(L, L_Y + a0, 16384), xf[0], f32x4{0.f, 0.f, 0.f, 0.f}), u1 = mfma(trfrag(L, L_Y + a1, 16384), xf[0], f32x4{0.f, 0.f, 0.f, 0.f});
        const bool hi = lj & 1;
        const f32x4 ga = lj < 2 ? g0 : g1;
        float ge = hi ? ga[2] : ga[0], go = hi ? ga[3] : ga[1];
        PIN(ge); PIN(go);
        wait_ge(FL + F_PROG, 160);
        STAMP(2);
        u0 = mfma(trfrag(L, L_Y + 32768 + a0, 16384), xf[1], u0); u1 = mfma(trfrag(L, L_Y + 32768 + a1, 16384), xf[1], u1);
        const f32x4 ua = lj < 2 ? u0 : u1;
        if (lj < 4) ST(pack4(ge, hi ? ua[2] : ua[0], go, hi ? ua[3] : ua[1], tagw), dst);
        STAMP(3);
        if (wave < 8) { STAMP_FLUSH(); return; }
    }
    {
        const int fw = wave - 8;
        const unsigned tb = (att ? L_Y : L_X) + rbase;
        unsigned a[4], dst = (((att ? 0 : 64) + ks) * 1024 + ct2 * 512 + fw * 64 + lj * 16 + lg * 4) * 4 + WS_S3;
        unsigned keep = (att || lg < 2) ? 0xffffffffu : 0u;
#pragma unroll
        for (int t = 0; t < 4; ++t) a[t] = tb + (((4 * fw + t) ^ key) << 5);
        PIN(dst); PIN(keep);
        v4u af[4], ag[4];
        wait_ge(FL + F_PROG, (att ? 96 : 160) + 32);
#pragma unroll
        for (int t = 0; t < 4; ++t) af[t] = trfrag(L, a[t], 16384);
        BARRIER();
        STAMP(6);
        const v4u h0 = xfrag(L, L_O + lg * 8), h1 = xfrag(L, L_O + lg * 8 + 64);
#pragma unroll
        for (int t = 0; t < 4; ++t) ag[t] = trfrag(L, a[t] + 32768, 16384);
        __builtin_amdgcn_sched_barrier(0);
        f32x4 c[4];
#pragma unroll
        for (int t = 0; t < 4; ++t) c[t] = mfma(af[t], h0, f32x4{0.f, 0.f, 0.f, 0.f});
#pragma unroll
        for (int t = 0; t < 4; ++t) { ag[t].z &= keep; ag[t].w &= keep; c[t] = mfma(ag[t], h1, c[t]); }
        const f32x4 cs = lj == 0 ? c[0] : lj == 1 ? c[1] : lj == 2 ? c[2] : c[3];
        if (lj < 4) ST(pack4(cs[0], cs[1], cs[2], cs[3], tagw), dst);
        STAMP(7);
    }
    STAMP_FLUSH();
}

extern "C" void kernel_launch(void* const* d_in, const int* in_sizes, int n_in, void* d_out, int out_size, void* d_ws, size_t ws_size,
                              hipStream_t stream) {
    (void)in_sizes; (void)n_in; (void)out_size; (void)ws_size;
    (void)hipFuncSetAttribute((const void*)fused_decode, hipFuncAttributeMaxDynamicSharedMemorySize, (int)LDS_BYTES);
    fused_decode<<<256, 1024, LDS_BYTES, stream>>>((const u16*)d_in[3], (const u16*)d_in[0], (unsigned char*)d_ws + WS_OFF, (const u16*)d_in[1],
                                                   (const u16*)d_in[2], (const u16*)d_in[4], (const u16*)d_in[6], (const u16*)d_in[7],
                                                   (const u16*)d_in[8], (u16*)d_out);
}
```

```cpp
#define PROBE 3
#include <hip/hip_runtime.h>
#include <hip/hip_bf16.h>
#include <stdint.h>

#define LAS __attribute__((address_space(3)))
#define GAS __attribute__((address_space(1)))
typedef unsigned short u16;
typedef unsigned v4u __attribute__((ext_vector_type(4)));
typedef unsigned v2u __attribute__((ext_vector_type(2)));
typedef int v4i __attribute__((ext_vector_type(4)));
typedef float f32x4 __attribute__((ext_vector_type(4)));
typedef float f32x2 __attribute__((ext_vector_type(2)));
typedef short s16x4 __attribute__((ext_vector_type(4)));
typedef __bf16 bf16x8 __attribute__((ext_vector_type(8)));
typedef __bf16 bf16x2 __attribute__((ext_vector_type(2)));

constexpr int DM = 1024, SK = 32768, WIN = 4096, FF = 3584;
constexpr unsigned WS_EPOCH = 0;
constexpr unsigned WS_S1 = 4096;
constexpr unsigned WS_SH = WS_S1 + 4 * 4096 * 4;
constexpr unsigned WS_S2O = WS_SH + 16 * 3584 * 8;
constexpr unsigned WS_S2M = WS_S2O + 64 * 32 * 256;
constexpr unsigned WS_S3 = WS_S2M + 32 * 32 * 16;
constexpr unsigned WS_END = WS_S3 + 128 * 1024 * 4;
constexpr unsigned L_X = 0;
constexpr unsigned L_Y = 65536;
constexpr unsigned L_XN = 131072;
constexpr unsigned L_Q = L_XN + 2048;
constexpr unsigned L_P = L_Q + 2304;
constexpr unsigned L_ML = L_P + 4096;
constexpr unsigned L_O = L_ML + 1024;
constexpr unsigned L_FLAG = L_O + 128;
constexpr unsigned L_JUNK = L_FLAG + 64;
constexpr unsigned L_TS = L_JUNK + 1024;
constexpr unsigned LDS_BYTES = L_TS + 1024;
enum { F_PROG = 0, F_XN = 1, F_TAG = 2, F_WQDONE = 3, F_GDONE = 4 };

#define D_A 28
#define D_W 16
#define W_PRE 4
#define W_DELAY 0
#define S3_SLEEP 26
#define D_B 32
#define D_D 28
#define DN_GATE 0
#define DN_PRE 0
#define DN_DELAY 4
#define WS_OFF 0
#define SH_SLEEP 0
#define S1_AT (96 - D_A)
#define SH_AT 32
#define S2_SLEEP 48
constexpr unsigned LDS_SPIN = 1u << 20, GLB_SPIN = 1u << 16;
constexpr int AUX_SC1 = 16;
constexpr float LOG2E = 1.4426950408889634f;

__device__ __forceinline__ float bflo(unsigned u) { return __builtin_bit_cast(float, u << 16); }
__device__ __forceinline__ float bfhi(unsigned u) { return __builtin_bit_cast(float, u & 0xffff0000u); }
__device__ __forceinline__ float rbf(float f) { return (float)(__bf16)f; }
__device__ __forceinline__ unsigned pkbf(float lo, float hi) { bf16x2 v = {(__bf16)lo, (__bf16)hi}; return __builtin_bit_cast(unsigned, v); }
__device__ __forceinline__ unsigned fu(float f) { return __builtin_bit_cast(unsigned, f); }
__device__ __forceinline__ float uf(unsigned u) { return __builtin_bit_cast(float, u); }
__device__ __forceinline__ float fmx(float a, float b) { return __builtin_elementwise_maximum(a, b); }
__device__ __forceinline__ unsigned g_lo(float a, float b) { return __builtin_amdgcn_perm(fu(b), fu(a), 0x05030201u); }
__device__ __forceinline__ unsigned g_hi(float b, unsigned tagw) { return __builtin_amdgcn_perm(tagw, fu(b), 0x07060302u); }
__device__ __forceinline__ v4u pack4(float a, float b, float c, float d, unsigned tagw) { return v4u{g_lo(a, b), g_hi(b, tagw), g_lo(c, d), g_hi(d, tagw)}; }
__device__ __forceinline__ float g_a(unsigned lo) { return uf(lo << 8); }
__device__ __forceinline__ float g_b(unsigned lo, unsigned hi) { return uf(__builtin_amdgcn_perm(hi, lo, 0x0504030cu)); }
__device__ __forceinline__ bool tag_ok(v4u r, unsigned tagw) { return (((r.y ^ tagw) | (r.w ^ tagw)) >> 16) == 0u; }

template <int CTRL> __device__ __forceinline__ float dppm(float v) {
    return __builtin_bit_cast(float, __builtin_amdgcn_mov_dpp(__builtin_bit_cast(int, v), CTRL, 0xf, 0xf, true));
}
__device__ __forceinline__ float row_sum(float v) {
    v += dppm<0xB1>(v); v += dppm<0x4E>(v); v += dppm<0x141>(v); v += dppm<0x128>(v);
    return v;
}
__device__ __forceinline__ float half_sum(float x) {
    auto t = __builtin_amdgcn_permlane32_swap(fu(x), fu(x), false, false);
    return uf(t[0]) + uf(t[1]);
}
__device__ __forceinline__ float xrow_max(float x) {
    auto s = __builtin_amdgcn_permlane16_swap(fu(x), fu(x), false, false);
    x = fmx(uf(s[0]), uf(s[1]));
    auto t = __builtin_amdgcn_permlane32_swap(fu(x), fu(x), false, false);
    return fmx(uf(t[0]), uf(t[1]));
}
__device__ __forceinline__ float xrow_sum(float x) {
    auto s = __builtin_amdgcn_permlane16_swap(fu(x), fu(x), false, false);
    return half_sum(uf(s[0]) + uf(s[1]));
}
__device__ __forceinline__ float wave_sum(float v) { return xrow_sum(row_sum(v)); }
__device__ __forceinline__ float wave_max(float x) {
    x = fmx(x, dppm<0xB1>(x)); x = fmx(x, dppm<0x4E>(x)); x = fmx(x, dppm<0x141>(x)); x = fmx(x, dppm<0x128>(x));
    return xrow_max(x);
}
__device__ __forceinline__ f32x4 mfma(v4u a, v4u b, f32x4 c) {
    return __builtin_amdgcn_mfma_f32_16x16x32_bf16(__builtin_bit_cast(bf16x8, a), __builtin_bit_cast(bf16x8, b), c, 0, 0, 0);
}
__device__ __forceinline__ f32x4 mfma16(v2u a, v2u b, f32x4 c) {
    return __builtin_amdgcn_mfma_f32_16x16x16bf16_1k(__builtin_bit_cast(s16x4, a), __builtin_bit_cast(s16x4, b), c, 0, 0, 0);
}
__device__ __forceinline__ v2u trread(LAS unsigned char* L, unsigned off) {
    return __builtin_bit_cast(v2u, __builtin_amdgcn_ds_read_tr16_b64_v4i16((LAS s16x4*)(L + off)));
}
__device__ __forceinline__ v4u trfrag(LAS unsigned char* L, unsigned off, unsigned toff) {
    const v2u a = trread(L, off), b = trread(L, off + toff);
    return v4u{a.x, a.y, b.x, b.y};
}
__device__ __forceinline__ v4u xfrag(LAS unsigned char* L, unsigned off) {
    const v2u a = *(LAS v2u*)(L + off), b = *(LAS v2u*)(L + off + 32);
    return v4u{a.x, a.y, b.x, b.y};
}
#define CBAR() asm volatile("" ::: "memory")
#define LGKM0() asm volatile("s_waitcnt lgkmcnt(0)" ::: "memory")
#define BARRIER() asm volatile("s_waitcnt lgkmcnt(0)\n\ts_barrier" ::: "memory")
#define PIN(v) asm volatile("" : "+v"(v))
__device__ __forceinline__ void wait_ge(volatile LAS unsigned* w, unsigned want) {
#pragma nounroll
    for (unsigned s = 0; s < LDS_SPIN; ++s) {
        if ((unsigned)__builtin_amdgcn_readfirstlane(*w) >= want) break;
        __builtin_amdgcn_s_sleep(1);
    }
    CBAR();
}
#define SIGNAL(flag) asm volatile("s_waitcnt lgkmcnt(0)\n\tds_add_u32 %0, %1 offset:%2" ::"v"(lane == 0 ? L_FLAG : L_JUNK + 4u * lane), "v"(1u), "i"(4 * (flag)) : "memory")
__device__ __forceinline__ v4i mkrsrc(const void* p, unsigned bytes) {
    const uint64_t a = (uint64_t)p;
    return v4i{(int)(unsigned)a, (int)((unsigned)(a >> 32) & 0xffffu), (int)bytes, 0x00020000};
}
#define VMWAIT(n) asm volatile("s_waitcnt vmcnt(%0)" ::"i"(n) : "memory")
#define LD(voff, soff) __builtin_amdgcn_raw_buffer_load_b128(wsr, (voff), (soff), AUX_SC1)
#define ST(v, voff) __builtin_amdgcn_raw_buffer_store_b128((v), wsr, (voff), 0, AUX_SC1)

#define STAMP(i) do { } while (0)
#define STAMP_FLUSH() do { } while (0)
#define STAMP_OUT() do { } while (0)

template <int DEP>
__device__ __forceinline__ void dma4(v4i rs, v4u v, unsigned& soff, unsigned sstep, unsigned lds) {
    asm volatile("s_mov_b32 m0, %[lds]\n\t"
                 "s_nop 0\n\t"
                 "buffer_load_dwordx4 %[v0], %[rs], %[so] offen nt lds\n\t"
                 "s_add_u32 m0, m0, 0x400\n\t"
                 "s_add_u32 %[so], %[so], %[st]\n\t"
                 "buffer_load_dwordx4 %[v1], %[rs], %[so] offen nt lds\n\t"
                 "s_add_u32 m0, m0, 0x400\n\t"
                 "s_add_u32 %[so], %[so], %[st]\n\t"
                 "s_waitcnt vmcnt(%[dep])\n\t"
                 "buffer_load_dwordx4 %[v2], %[rs], %[so] offen nt lds\n\t"
                 "s_add_u32 m0, m0, 0x400\n\t"
                 "s_add_u32 %[so], %[so], %[st]\n\t"
                 "buffer_load_dwordx4 %[v3], %[rs], %[so] offen nt lds\n\t"
                 "s_add_u32 %[so], %[so], %[st]\n\t"
                 "s_waitcnt vmcnt(%[dep])"
                 : [so] "+s"(soff)
                 : [lds] "s"(lds), [v0] "v"(v.x), [v1] "v"(v.y), [v2] "v"(v.z), [v3] "v"(v.w), [rs] "s"(rs), [st] "s"(sstep), [dep] "i"(DEP)
                 : "memory", "scc");
}
template <int DEP>
__device__ __forceinline__ void stream(v4i rs, v4u va, v4u vb, unsigned soff, unsigned sstep, unsigned lds, int ng, int& prog, unsigned progaddr) {
    const unsigned end = lds + ng * 4096;
#pragma nounroll
    do {
        dma4<DEP>(rs, va, soff, sstep, lds);
        prog += 4;
        asm volatile("ds_max_i32 %0, %1" ::"v"(progaddr), "v"(prog - DEP) : "memory");
        dma4<DEP>(rs, vb, soff, sstep, lds + 4096);
        prog += 4; lds += 8192;
        asm volatile("ds_max_i32 %0, %1" ::"v"(progaddr), "v"(prog - DEP) : "memory");
    } while (lds != end);
}

__global__ void __launch_bounds__(1024, 4)
fused_decode(const u16* __restrict__ wq, const u16* __restrict__ x, unsigned char* ws, const u16* __restrict__ kc, const u16* __restrict__ vc,
             const u16* __restrict__ wo, const u16* __restrict__ wg, const u16* __restrict__ wu, const u16* __restrict__ wd, u16* out) {
    extern __shared__ __attribute__((aligned(16))) unsigned char smem[];
    LAS unsigned char* L = (LAS unsigned char*)smem;
    volatile LAS unsigned* FL = (volatile LAS unsigned*)(L + L_FLAG);
    const int tid = threadIdx.x, lane = tid & 63, wave = __builtin_amdgcn_readfirstlane(tid >> 6), b = blockIdx.x;
    STAMP(0);
    const unsigned long long pt0 = __builtin_amdgcn_s_memrealtime();
    FL[lane & 15] = 0u;
    v4u xa, xb;
    unsigned ep;
    {
        const int rl = lane >> 3, p = lane & 7;
        const unsigned v0 = rl * 8192 + (((p >> 1) ^ ((rl >> 1) & 3)) << 5) + (p & 1) * 16;
        unsigned so = (unsigned)(b >> 6) * 256 * 8192 + (b & 63) * 128;
        const v4i rs = mkrsrc(wq, DM * 4096 * 2);
        const unsigned l16 = lane * 16, zero = 0;
        uint64_t pc;
        unsigned coff = (b >> 3) * 1024 + l16;
        asm volatile("s_cmp_lg_u32 %[wave], 0\n\t"
                     "s_cbranch_scc1 1f\n\t"
                     "s_mov_b32 m0, %[lds]\n"
                     "0:\n\t"
                     "s_nop 0\n\t"
                     "buffer_load_dwordx4 %[v0], %[rs], %[so] offen nt lds\n\t"
                     "s_add_u32 m0, m0, 0x400\n\t"
                     "s_add_u32 %[so], %[so], 0x10000\n\t"
                     "buffer_load_dwordx4 %[v0], %[rs], %[so] offen nt lds\n\t"
                     "s_add_u32 m0, m0, 0x400\n\t"
                     "s_add_u32 %[so], %[so], 0x10000\n\t"
                     "buffer_load_dwordx4 %[v0], %[rs], %[so] offen nt lds\n\t"
                     "s_add_u32 m0, m0, 0x400\n\t"
                     "s_add_u32 %[so], %[so], 0x10000\n\t"
                     "buffer_load_dwordx4 %[v0], %[rs], %[so] offen nt lds\n\t"
                     "s_add_u32 m0, m0, 0x400\n\t"
                     "s_add_u32 %[so], %[so], 0x10000\n\t"
                     "s_cmp_lg_u32 m0, %[lend]\n\t"
                     "s_cbranch_scc1 0b\n\t"
                     "s_branch 3f\n"
                     "1:\n\t"
                     "s_cmp_lg_u32 %[wave], 1\n\t"
                     "s_cbranch_scc1 2f\n\t"
                     "global_load_dwordx4 %[xa], %[l16], %[x]\n\t"
                     "global_load_dwordx4 %[xb], %[l16], %[x] offset:1024\n\t"
                     "global_load_dword %[ep], %[zero], %[ws] sc1\n\t"
                     "s_branch 3f\n"
                     "2:\n\t"
                     "s_cmp_lg_u32 %[wave], 2\n\t"
                     "s_cbranch_scc1 3f\n\t"
                     "s_mov_b32 m0, %[junk]\n\t"
                     ".subsection 1\n"
                     "5:\n\t"
                     ".subsection 0\n\t"
                     "v_min_u32 %[coff], 5b-4f-16, %[coff]\n\t"
                     "s_getpc_b64 %[pc]\n"
                     "4:\n\t"
                     "global_load_lds_dwordx4 %[coff], %[pc]\n"
                     "3:\n\t"
                     "s_waitcnt lgkmcnt(0)\n\t"
                     "s_barrier"
                     : [so] "+s"(so), [xa] "=&v"(xa), [xb] "=&v"(xb), [ep] "=&v"(ep), [pc] "=&s"(pc), [coff] "+v"(coff)
                     : [wave] "s"(wave), [lds] "i"(L_Y), [lend] "i"(L_Y + 24576), [junk] "i"(L_JUNK), [v0] "v"(v0), [rs] "s"(rs), [l16] "v"(l16), [zero] "v"(zero), [x] "s"(x), [ws] "s"(ws),
                       "s"(kc), "s"(vc), "s"(wo), "s"(wg), "s"(wu), "s"(wd), "s"(out)
                     : "memory", "scc");
        const u16 *p1 = (b & 8) ? kc : wg, *p2 = (b & 8) ? vc : wu, *p3 = (b & 8) ? wo : wd;
        if (wave == 0) {
            const int xc = b & 7, bi = b >> 3, idx = xc * 16 + (bi >> 1), ks = idx >> 1, ct2 = idx & 1;
            const int lj = lane & 15, lg = lane >> 4;
            const unsigned pa = lane == 0 ? L_FLAG + 4 * F_PROG : L_JUNK + 4 * lane;
            int cnt = 24;
            PIN(cnt);

            stream<28>(rs, v4u{v0, v0, v0, v0}, v4u{v0, v0, v0, v0}, so, 65536, L_Y + 24576, 2, cnt, pa);
            STAMP(1);
            v4u va, vb;
#pragma unroll
            for (int i = 0; i < 4; ++i) { va[i] = l16 ^ (i << 5); vb[i] = l16 ^ ((i + 4) << 5); }
            if (bi & 1) {
                const size_t kvoff = ((size_t)(xc >> 1) * SK + (SK - WIN) + (idx & 31) * 128) * 128;
                const unsigned k0 = lg * 256 + ((lj ^ lg) << 4);
                const v4u kv = {k0, k0 ^ 0x40u, k0 ^ 0x80u, k0 ^ 0xC0u};
                stream<D_A>(mkrsrc(p1 + kvoff, 32768), kv, kv, 0, 1024, L_X, 8, cnt, pa);
                const unsigned u0 = lg * 256 + (((lj >> 1) ^ lg) << 5) + (lj & 1) * 16;
                const v4u vv = {u0, u0 ^ 0x80u, u0, u0 ^ 0x80u};
                stream<D_A>(mkrsrc(p2 + kvoff, 32768), vv, vv, 0, 1024, L_X + 32768, 8, cnt, pa);
                STAMP(2);
                VMWAIT(0);
                STAMP(3);
                BARRIER();
                const v4i wr = mkrsrc(p3, 4096 * DM * 2);
                const unsigned so2 = (unsigned)((ks >> 2) * 256 + (ks & 3) * 32) * 2048 + ct2 * 1024;
                if (W_PRE) stream<D_W>(wr, va, vb, so2, 2048, L_Y, W_PRE, cnt, pa);
                BARRIER();
                if (W_DELAY) __builtin_amdgcn_s_sleep(W_DELAY);
                stream<D_W>(wr, va, vb, so2 + W_PRE * 4 * 2048, 2048, L_Y + W_PRE * 4096, 8 - W_PRE, cnt, pa);
                stream<D_W>(wr, va, vb, so2 + 128 * 2048, 2048, L_Y + 32768, 8, cnt, pa);
            } else {
                v4u ga, gb;
#pragma unroll
                for (int i = 0; i < 4; ++i) { ga[i] = va[i] < 896 ? va[i] : 0x40000000u; gb[i] = vb[i] < 896 ? vb[i] : 0x40000000u; }
                const unsigned so2 = (unsigned)(idx >> 3) * 64 * 7168 + (idx & 7) * 896;
                stream<D_B>(mkrsrc(p1, DM * FF * 2), ga, gb, so2, 7168, L_X, 16, cnt, pa);
                STAMP(2);
                wait_ge(FL + F_WQDONE, 2);
                stream<D_B>(mkrsrc(p2, DM * FF * 2), ga, gb, so2, 7168, L_Y, 16, cnt, pa);
                STAMP(3);
                wait_ge(FL + F_GDONE, 14);
                const v4i dr = mkrsrc(p3, FF * DM * 2);
                const unsigned so3 = (unsigned)ks * 56 * 2048 + ct2 * 1024;
                stream<D_D>(dr, va, vb, so3, 2048, L_X, 14, cnt, pa);
                VMWAIT(24);
                asm volatile("ds_max_i32 %0, %1" ::"v"(pa), "v"(cnt - 24) : "memory");
            }
            STAMP(4);
            VMWAIT(0);
            STAMP(5);
            BARRIER();
            STAMP_FLUSH();
            return;
        }
    }
    const int xc = b & 7, bi = b >> 3;
    const bool att = bi & 1;
    const int idx = xc * 16 + (bi >> 1);
    const int ks = idx >> 1, ct2 = idx & 1;
    const int lj = lane & 15, lg = lane >> 4;
    const int q4 = lj >> 2, p4 = lane & 3;
    const unsigned key = (unsigned)(q4 | ((lg & 1) << 2));
    const __amdgpu_buffer_rsrc_t wsr = __builtin_amdgcn_make_buffer_rsrc((void*)ws, 0, (int)WS_END, 0x00020000);

    if (wave == 1) {
        asm volatile("s_waitcnt vmcnt(0)" : "+v"(xa), "+v"(xb), "+v"(ep)::"memory");
        float v[16];
#pragma unroll
        for (int i = 0; i < 4; ++i) { v[2 * i] = bflo(xa[i]); v[2 * i + 1] = bfhi(xa[i]); v[8 + 2 * i] = bflo(xb[i]); v[9 + 2 * i] = bfhi(xb[i]); }
        float s = 0.f;
#pragma unroll
        for (int i = 0; i < 16; ++i) s += v[i];
        const float mean = wave_sum(s) * (1.0f / DM);
        float q = 0.f;
#pragma unroll
        for (int i = 0; i < 16; ++i) { const float d = v[i] - mean; q = fmaf(d, d, q); }
        const float var = wave_sum(q) * (1.0f / DM);
        const float mb = rbf(mean), den = rbf(__builtin_amdgcn_sqrtf(rbf(rbf(var) + 1e-5f)));
        const float rden = __builtin_amdgcn_rcpf(den);
        v4u oa, ob;
#pragma unroll
        for (int i = 0; i < 4; ++i) {
            const unsigned da = pkbf(v[2 * i] - mb, v[2 * i + 1] - mb), db = pkbf(v[8 + 2 * i] - mb, v[9 + 2 * i] - mb);
            oa[i] = pkbf(bflo(da) * rden, bfhi(da) * rden);
            ob[i] = pkbf(bflo(db) * rden, bfhi(db) * rden);
        }
        *(LAS v4u*)(L + L_XN + lane * 16) = oa;
        *(LAS v4u*)(L + L_XN + 1024 + lane * 16) = ob;
        const unsigned tag = ep + 1u, tagw = tag << 16;
        if (lane == 0) FL[F_TAG] = tag;
        SIGNAL(F_XN);
        STAMP(1);
        const int own = xc * 32 + bi, col = own * 4 + ((lg & 1) << 1) + (lg >> 1);
        const float xres = bflo(x[col]);
        u16* outp = out + col;
        asm volatile("" : "+v"(outp));
        if (att) {
            BARRIER();
            BARRIER();
            __builtin_amdgcn_s_sleep(S2_SLEEP);
            STAMP(2);
            v4u r[8], mlv;
            bool ok[8], okm = false;
#pragma unroll
            for (int i = 0; i < 8; ++i) ok[i] = false;
#pragma nounroll
            for (unsigned sp = 0; sp < GLB_SPIN; ++sp) {
                if (!okm) mlv = LD(((ks >> 4) * 32 + (lane & 31)) * 128 + (((ks >> 2) & 3) * 2 + (lane >> 5)) * 16, WS_S2M);
#pragma unroll
                for (int i = 0; i < 8; ++i) if (!ok[i]) r[i] = LD(ks * 8192 + lane * 16, WS_S2O + i * 1024);
                okm = (mlv.y == tag) & (mlv.w == tag);
                bool all = okm;
#pragma unroll
                for (int i = 0; i < 8; ++i) { ok[i] = tag_ok(r[i], tagw); all &= ok[i]; }
                if (__all(all)) break;
                CBAR();
            }
            STAMP(3);
            float Mx = uf(mlv.x);
            Mx = fmx(Mx, dppm<0xB1>(Mx)); Mx = fmx(Mx, dppm<0x4E>(Mx)); Mx = fmx(Mx, dppm<0x141>(Mx)); Mx = fmx(Mx, dppm<0x128>(Mx));
            { auto t = __builtin_amdgcn_permlane16_swap(fu(Mx), fu(Mx), false, false); Mx = fmx(uf(t[0]), uf(t[1])); }
            float wgt = __builtin_amdgcn_exp2f(uf(mlv.x) - Mx);
            float Ls = row_sum(wgt * uf(mlv.z));
            { auto t = __builtin_amdgcn_permlane16_swap(fu(Ls), fu(Ls), false, false); Ls = uf(t[0]) + uf(t[1]); }
            wgt *= __builtin_amdgcn_rcpf(Ls);
            f32x4 acc = {0.f, 0.f, 0.f, 0.f};
#pragma unroll
            for (int i = 0; i < 8; ++i) {
                const float w = uf(__builtin_amdgcn_ds_bpermute((4 * i + lg + (lj & 4) * 8) * 4, fu(wgt)));
                acc[0] = fmaf(w, g_a(r[i].x), acc[0]); acc[1] = fmaf(w, g_b(r[i].x, r[i].y), acc[1]);
                acc[2] = fmaf(w, g_a(r[i].z), acc[2]); acc[3] = fmaf(w, g_b(r[i].z, r[i].w), acc[3]);
            }
#pragma unroll
            for (int k = 0; k < 4; ++k) acc[k] = xrow_sum(acc[k]);
            if (lane < 16) *(LAS v2u*)(L + L_O + (lj & 4) * 16 + (lj >> 3) * 32 + (lj & 3) * 8) = v2u{pkbf(acc[0], acc[1]), pkbf(acc[2], acc[3])};
        } else {
            wait_ge(FL + F_PROG, 160 + SH_AT);
            if (SH_SLEEP) __builtin_amdgcn_s_sleep(SH_SLEEP);
            STAMP(2);
            const int un = lane & 31;
            const unsigned off = ((lane >> 5) * FF + ks * 56 + (un < 28 ? un : 27) * 2) * 8;
            v4u r[8];
            bool ok[8];
#pragma unroll
            for (int i = 0; i < 8; ++i) ok[i] = false;
#pragma nounroll
            for (unsigned sp = 0; sp < GLB_SPIN; ++sp) {
#pragma unroll
                for (int i = 0; i < 8; ++i) if (!ok[i]) r[i] = LD(off, WS_SH + i * 2 * FF * 8);
                bool all = true;
#pragma unroll
                for (int i = 0; i < 8; ++i) { ok[i] = tag_ok(r[i], tagw); all &= ok[i]; }
                if (__all(all)) break;
                CBAR();
            }
            STAMP(3);
            float hv[2];
#pragma unroll
            for (int c = 0; c < 2; ++c) {
                float g = 0.f, u = 0.f;
#pragma unroll
                for (int i = 0; i < 8; ++i) { g += g_a(r[i][2 * c]); u += g_b(r[i][2 * c], r[i][2 * c + 1]); }
                g = rbf(half_sum(g)); u = rbf(half_sum(u));
                hv[c] = rbf(g * __builtin_amdgcn_rcpf(1.0f + __builtin_amdgcn_exp2f(-g * LOG2E))) * u;
            }
            if (lane < 32) *(LAS unsigned*)(L + L_O + lane * 4) = lane < 28 ? pkbf(hv[0], hv[1]) : 0u;
        }
        STAMP(4);
        BARRIER();
        __builtin_amdgcn_s_sleep(S3_SLEEP);
        v4u r0, r1;
        bool ok0 = false, ok1 = false;
#pragma nounroll
        for (unsigned sp = 0; sp < GLB_SPIN; ++sp) {
            if (!ok0) r0 = LD(lane * 4096 + own * 16, WS_S3);
            if (!ok1) r1 = LD(lane * 4096 + own * 16, WS_S3 + 64 * 4096);
            ok0 = tag_ok(r0, tagw); ok1 = tag_ok(r1, tagw);
            if (__all(ok0 & ok1)) break;
            CBAR();
        }
        STAMP(5);
        const float s0 = g_a(r0.x) + g_a(r1.x), s1 = g_b(r0.x, r0.y) + g_b(r1.x, r1.y);
        const float s2 = g_a(r0.z) + g_a(r1.z), s3 = g_b(r0.z, r0.w) + g_b(r1.z, r1.w);
        auto t01 = __builtin_amdgcn_permlane32_swap(fu(s0), fu(s1), false, false);
        auto t23 = __builtin_amdgcn_permlane32_swap(fu(s2), fu(s3), false, false);
        auto t = __builtin_amdgcn_permlane16_swap(fu(uf(t01[0]) + uf(t01[1])), fu(uf(t23[0]) + uf(t23[1])), false, false);
        const float tot = row_sum(uf(t[0]) + uf(t[1])) + xres;
        if (lj == 0) {
            *(GAS __bf16*)outp = (__bf16)tot;
            if (b == 0 && lane == 0) __hip_atomic_store((GAS unsigned*)(ws + WS_EPOCH), tag, __ATOMIC_RELAXED, __HIP_MEMORY_SCOPE_AGENT);
        }
        if ((b & 7) == PROBE && b < 8) { while (__builtin_amdgcn_s_memrealtime() - pt0 < 3000) __builtin_amdgcn_s_sleep(1); }
        STAMP(6);
        STAMP_FLUSH();
        STAMP_OUT();
        return;
    }
    if (att && wave < 6) { STAMP_FLUSH(); return; }
    wait_ge(FL + F_XN, 1);
    const unsigned tag = FL[F_TAG], tagw = tag << 16;
    if ((wave & 14) == 6) {
        const int c = wave & 1;
        const unsigned row = 4 * lg + q4;
        const unsigned a0 = L_Y + row * 128 + (((2 * c) ^ ((row >> 1) & 3)) << 5) + p4 * 8, a1 = a0 ^ 32u;
        const unsigned xoff = L_XN + (b >> 6) * 512 + lg * 8;
        unsigned dst = ((b >> 6) * 4096 + (b & 63) * 64 + c * 32 + lj * 16 + lg * 4) * 4 + WS_S1;
        PIN(dst);
        f32x4 acc0 = {0.f, 0.f, 0.f, 0.f}, acc1 = {0.f, 0.f, 0.f, 0.f};
#pragma nounroll
        for (int h = 0; h < 4; ++h) {
            v4u xq[2], af[2], ag[2];
#pragma unroll
            for (int s = 0; s < 2; ++s) xq[s] = xfrag(L, xoff + h * 128 + s * 64);
            wait_ge(FL + F_PROG, 8 * h + 8);
#pragma unroll
            for (int s = 0; s < 2; ++s) { af[s] = trfrag(L, a0 + h * 8192 + s * 4096, 2048); ag[s] = trfrag(L, a1 + h * 8192 + s * 4096, 2048); }
            __builtin_amdgcn_sched_barrier(0);
#pragma unroll
            for (int s = 0; s < 2; ++s) { acc0 = mfma(af[s], xq[s], acc0); acc1 = mfma(ag[s], xq[s], acc1); }
        }
        const f32x4 acc = lj == 0 ? acc0 : acc1;
        if (lj < 2) ST(pack4(acc[0], acc[1], acc[2], acc[3], tagw), dst);
        SIGNAL(F_WQDONE);
        STAMP(1);
        if (att) { STAMP_FLUSH(); return; }
    }
    const unsigned rbase = (4 * lg + q4) * 1024 + p4 * 8;
    if (att) {
        const int aw = wave - 8, gkv = xc >> 1, split = idx & 31;
        unsigned a_q = L_Q + (lj & 7) * 272 + lg * 16, a_k = L_X + (16 * aw + lj) * 256, a_v = L_X + 32768 + (4 * lg + q4) * 256 + ((aw ^ key) << 5) + p4 * 8;
        unsigned a_p = L_P + lane * 8, a_ml = L_ML + (lj & 7) * 80;
        unsigned dst = (((gkv * 4 + (lj >> 1)) * 4 + (aw >> 1)) * 32 + split) * 256 + (aw & 1) * 128 + (lj & 1) * 64 + lg * 16 + WS_S2O;
        unsigned k0 = a_k + ((lg ^ lj) << 4), k1 = a_k + (((4 + lg) ^ lj) << 4), k2 = a_k + (((8 + lg) ^ lj) << 4), k3 = a_k + (((12 + lg) ^ lj) << 4);
        PIN(a_q); PIN(a_v); PIN(a_p); PIN(a_ml); PIN(dst);
        wait_ge(FL + F_PROG, S1_AT);
        STAMP(1);
        v4u kf[4];
        kf[0] = *(LAS v4u*)(L + k0); kf[1] = *(LAS v4u*)(L + k1); kf[2] = *(LAS v4u*)(L + k2); kf[3] = *(LAS v4u*)(L + k3);
        {
            v4u r0, r1;
#pragma nounroll
            for (unsigned sp = 0; sp < GLB_SPIN; ++sp) {
                r0 = LD((lane >> 5) * 16384 + gkv * 4096 + aw * 512 + (lane & 31) * 16, WS_S1);
                r1 = LD((lane >> 5) * 16384 + gkv * 4096 + aw * 512 + (lane & 31) * 16, WS_S1 + 32768);
                if (__all(tag_ok(r0, tagw) & tag_ok(r1, tagw))) break;
                CBAR();
            }
            STAMP(2);
            const float q0 = half_sum(g_a(r0.x) + g_a(r1.x)), q1 = half_sum(g_b(r0.x, r0.y) + g_b(r1.x, r1.y));
            const float q2 = half_sum(g_a(r0.z) + g_a(r1.z)), q3 = half_sum(g_b(r0.z, r0.w) + g_b(r1.z, r1.w));
            if (lane < 32) *(LAS v2u*)(L + L_Q + aw * 272 + lane * 8) = v2u{pkbf(q0, q1), pkbf(q2, q3)};
        }
        BARRIER();
        STAMP(3);
        v4u qf[4];
#pragma unroll
        for (int s = 0; s < 4; ++s) qf[s] = *(LAS v4u*)(L + a_q + s * 64);
        __builtin_amdgcn_sched_barrier(0);
        f32x4 sc = {0.f, 0.f, 0.f, 0.f};
#pragma unroll
        for (int s = 0; s < 4; ++s) sc = mfma(kf[s], qf[s], sc);
        __builtin_amdgcn_sched_barrier(0);
        v2u vf[8];
#pragma unroll
        for (int t = 0; t < 8; ++t) vf[t] = trread(L, a_v + t * 4096);
        __builtin_amdgcn_sched_barrier(0);
        {
            const unsigned r01 = pkbf(sc[0], sc[1]), r23 = pkbf(sc[2], sc[3]);
            const float SC = 0.08838834764831845f;
            const unsigned s01 = pkbf(bflo(r01) * SC, bfhi(r01) * SC), s23 = pkbf(bflo(r23) * SC, bfhi(r23) * SC);
            sc[0] = bflo(s01); sc[1] = bfhi(s01); sc[2] = bflo(s23); sc[3] = bfhi(s23);
        }
        const float m = xrow_max(fmx(fmx(sc[0], sc[1]), fmx(sc[2], sc[3]))) * LOG2E;
#pragma unroll
        for (int r = 0; r < 4; ++r) sc[r] = __builtin_amdgcn_exp2f(fmaf(sc[r], LOG2E, -m));
        *(LAS v2u*)(L + a_p + aw * 512) = v2u{pkbf(sc[0], sc[1]), pkbf(sc[2], sc[3])};
        const float l = xrow_sum((sc[0] + sc[1]) + (sc[2] + sc[3]));
        if (lane < 8) *(LAS v2u*)(L + a_ml + aw * 8) = v2u{fu(m), fu(l)};
        BARRIER();
        STAMP(4);
        v2u pf[8];
        f32x4 ml[4];
#pragma unroll
        for (int t = 0; t < 4; ++t) ml[t] = *(LAS f32x4*)(L + a_ml + t * 16);
#pragma unroll
        for (int t = 0; t < 8; ++t) pf[t] = *(LAS v2u*)(L + a_p + t * 512);
        __builtin_amdgcn_sched_barrier(0);
        f32x4 ot[8];
#pragma unroll
        for (int t = 0; t < 8; ++t) ot[t] = mfma16(vf[t], pf[t], f32x4{0.f, 0.f, 0.f, 0.f});
        const float M = fmx(fmx(fmx(ml[0][0], ml[0][2]), fmx(ml[1][0], ml[1][2])), fmx(fmx(ml[2][0], ml[2][2]), fmx(ml[3][0], ml[3][2])));
        f32x4 o = {0.f, 0.f, 0.f, 0.f};
        float lt = 0.f;
#pragma unroll
        for (int t = 0; t < 8; ++t) {
            const float w = __builtin_amdgcn_exp2f(ml[t >> 1][(t & 1) * 2] - M);
            lt = fmaf(w, ml[t >> 1][(t & 1) * 2 + 1], lt);
            o += w * ot[t];
        }
        if (lj < 8) ST(pack4(o[0], o[1], o[2], o[3], tagw), dst);
        if (aw == 0 && lane < 8) ST((v4u{fu(M), tag, fu(lt), tag}), (gkv * 32 + split) * 128 + lj * 16 + WS_S2M);
        STAMP(5);
    } else {
        const int gw = wave - 2;
        unsigned a0 = rbase + (((2 * gw) ^ key) << 5), a1 = rbase + (((2 * gw + 1) ^ key) << 5);
        unsigned dst = ((idx >> 3) * FF + (idx & 7) * 448 + gw * 32 + (lj >> 1) * 16 + lg * 4 + (lj & 1) * 2) * 8 + WS_SH;
        PIN(dst);
        v4u xf[2], af[2][2];
#pragma unroll
        for (int s = 0; s < 2; ++s) xf[s] = xfrag(L, L_XN + (idx >> 3) * 128 + lg * 8 + s * 64);
        wait_ge(FL + F_PROG, 96);
#pragma unroll
        for (int s = 0; s < 2; ++s) { af[s][0] = trfrag(L, L_X + a0 + s * 32768, 16384); af[s][1] = trfrag(L, L_X + a1 + s * 32768, 16384); }
        SIGNAL(F_GDONE);
        f32x4 g0 = mfma(af[0][0], xf[0], f32x4{0.f, 0.f, 0.f, 0.f}), g1 = mfma(af[0][1], xf[0], f32x4{0.f, 0.f, 0.f, 0.f});
        g0 = mfma(af[1][0], xf[1], g0); g1 = mfma(af[1][1], xf[1], g1);
        wait_ge(FL + F_PROG, 128);
        f32x4 u0 = mfma(trfrag(L, L_Y + a0, 16384), xf[0], f32x4{0.f, 0.f, 0.f, 0.f}), u1 = mfma(trfrag(L, L_Y + a1, 16384), xf[0], f32x4{0.f, 0.f, 0.f, 0.f});
        const bool hi = lj & 1;
        const f32x4 ga = lj < 2 ? g0 : g1;
        float ge = hi ? ga[2] : ga[0], go = hi ? ga[3] : ga[1];
        PIN(ge); PIN(go);
        wait_ge(FL + F_PROG, 160);
        STAMP(2);
        u0 = mfma(trfrag(L, L_Y + 32768 + a0, 16384), xf[1], u0); u1 = mfma(trfrag(L, L_Y + 32768 + a1, 16384), xf[1], u1);
        const f32x4 ua = lj < 2 ? u0 : u1;
        if (lj < 4) ST(pack4(ge, hi ? ua[2] : ua[0], go, hi ? ua[3] : ua[1], tagw), dst);
        STAMP(3);
        if (wave < 8) { STAMP_FLUSH(); return; }
    }
    {
        const int fw = wave - 8;
        const unsigned tb = (att ? L_Y : L_X) + rbase;
        unsigned a[4], dst = (((att ? 0 : 64) + ks) * 1024 + ct2 * 512 + fw * 64 + lj * 16 + lg * 4) * 4 + WS_S3;
        unsigned keep = (att || lg < 2) ? 0xffffffffu : 0u;
#pragma unroll
        for (int t = 0; t < 4; ++t) a[t] = tb + (((4 * fw + t) ^ key) << 5);
        PIN(dst); PIN(keep);
        v4u af[4], ag[4];
        wait_ge(FL + F_PROG, (att ? 96 : 160) + 32);
#pragma unroll
        for (int t = 0; t < 4; ++t) af[t] = trfrag(L, a[t], 16384);
        BARRIER();
        STAMP(6);
        const v4u h0 = xfrag(L, L_O + lg * 8), h1 = xfrag(L, L_O + lg * 8 + 64);
#pragma unroll
        for (int t = 0; t < 4; ++t) ag[t] = trfrag(L, a[t] + 32768, 16384);
        __builtin_amdgcn_sched_barrier(0);
        f32x4 c[4];
#pragma unroll
        for (int t = 0; t < 4; ++t) c[t] = mfma(af[t], h0, f32x4{0.f, 0.f, 0.f, 0.f});
#pragma unroll
        for (int t = 0; t < 4; ++t) { ag[t].z &= keep; ag[t].w &= keep; c[t] = mfma(ag[t], h1, c[t]); }
        const f32x4 cs = lj == 0 ? c[0] : lj == 1 ? c[1] : lj == 2 ? c[2] : c[3];
        if (lj < 4) ST(pack4(cs[0], cs[1], cs[2], cs[3], tagw), dst);
        STAMP(7);
    }
    STAMP_FLUSH();
}

extern "C" void kernel_launch(void* const* d_in, const int* in_sizes, int n_in, void* d_out, int out_size, void* d_ws, size_t ws_size,
                              hipStream_t stream) {
    (void)in_sizes; (void)n_in; (void)out_size; (void)ws_size;
    (void)hipFuncSetAttribute((const void*)fused_decode, hipFuncAttributeMaxDynamicSharedMemorySize, (int)LDS_BYTES);
    fused_decode<<<256, 1024, LDS_BYTES, stream>>>((const u16*)d_in[3], (const u16*)d_in[0], (unsigned char*)d_ws + WS_OFF, (const u16*)d_in[1],
                                                   (const u16*)d_in[2], (const u16*)d_in[4], (const u16*)d_in[6], (const u16*)d_in[7],
                                                   (const u16*)d_in[8], (u16*)d_out);
}
```

```cpp
#include <hip/hip_runtime.h>
#include <hip/hip_bf16.h>
#include <stdint.h>

#define LAS __attribute__((address_space(3)))
#define GAS __attribute__((address_space(1)))
typedef unsigned short u16;
typedef unsigned v4u __attribute__((ext_vector_type(4)));
typedef unsigned v2u __attribute__((ext_vector_type(2)));
typedef int v4i __attribute__((ext_vector_type(4)));
typedef float f32x4 __attribute__((ext_vector_type(4)));
typedef float f32x2 __attribute__((ext_vector_type(2)));
typedef short s16x4 __attribute__((ext_vector_type(4)));
typedef __bf16 bf16x8 __attribute__((ext_vector_type(8)));
typedef __bf16 bf16x2 __attribute__((ext_vector_type(2)));

constexpr int DM = 1024, SK = 32768, WIN = 4096, FF = 3584;
constexpr unsigned WS_EPOCH = 0;
constexpr unsigned WS_S1 = 4096;
constexpr unsigned WS_SH = WS_S1 + 4 * 4096 * 4;
constexpr unsigned WS_S2O = WS_SH + 16 * 3584 * 8;
constexpr unsigned WS_S2M = WS_S2O + 64 * 32 * 256;
constexpr unsigned WS_S3 = WS_S2M + 32 * 32 * 16;
constexpr unsigned WS_END = WS_S3 + 128 * 1024 * 4;
constexpr unsigned L_X = 0;
constexpr unsigned L_Y = 65536;
constexpr unsigned L_XN = 131072;
constexpr unsigned L_Q = L_XN + 2048;
constexpr unsigned L_P = L_Q + 2304;
constexpr unsigned L_ML = L_P + 4096;
constexpr unsigned L_O = L_ML + 1024;
constexpr unsigned L_FLAG = L_O + 128;
constexpr unsigned L_JUNK = L_FLAG + 64;
constexpr unsigned L_TS = L_JUNK + 1024;
constexpr unsigned LDS_BYTES = L_TS + 1024;
enum { F_PROG = 0, F_G = 1, F_TAG = 2, F_WQDONE = 3, F_GDONE = 4, F_XN = 5 };

#define D_A 28
#define D_W 20
#define W_PRE 4
#define W_DELAY 0
#define S3_SLEEP 26
#define D_B 32
#define D_D 24
#define DN_GATE 0
#define DN_PRE 0
#define DN_DELAY 4
#define WS_OFF 0
#define SH_SLEEP 8
#define D_W2 D_W
#define S2_T 0
#define SH_T 0
#define S3_T 0
#define OUT_MODE 0
#define EP_PLAIN 0
#define W_EARLY 0
#define S3W 16
#define OWN_ATT 1
#define ORDER 0
#define TLBPF 0
#define EP_SCALAR 0
#define PFMODE 0
#define S1_AT (96 - D_A)
#define SH_AT 32
#define S2_SLEEP 56
#define NOHO 0
#define NOSWZ 0
constexpr unsigned LDS_SPIN = 1u << 20, GLB_SPIN = NOHO ? 1u : 1u << 16;
constexpr int AUX_SC1 = 16;
constexpr float LOG2E = 1.4426950408889634f;

__device__ __forceinline__ float bflo(unsigned u) { return __builtin_bit_cast(float, u << 16); }
__device__ __forceinline__ float bfhi(unsigned u) { return __builtin_bit_cast(float, u & 0xffff0000u); }
__device__ __forceinline__ float rbf(float f) { return (float)(__bf16)f; }
__device__ __forceinline__ unsigned pkbf(float lo, float hi) { bf16x2 v = {(__bf16)lo, (__bf16)hi}; return __builtin_bit_cast(unsigned, v); }
__device__ __forceinline__ unsigned fu(float f) { return __builtin_bit_cast(unsigned, f); }
__device__ __forceinline__ float uf(unsigned u) { return __builtin_bit_cast(float, u); }
__device__ __forceinline__ float fmx(float a, float b) { return __builtin_elementwise_maximum(a, b); }
__device__ __forceinline__ unsigned g_lo(float a, float b) { return __builtin_amdgcn_perm(fu(b), fu(a), 0x05030201u); }
__device__ __forceinline__ unsigned g_hi(float b, unsigned tagw) { return __builtin_amdgcn_perm(tagw, fu(b), 0x07060302u); }
__device__ __forceinline__ v4u pack4(float a, float b, float c, float d, unsigned tagw) { return v4u{g_lo(a, b), g_hi(b, tagw), g_lo(c, d), g_hi(d, tagw)}; }
__device__ __forceinline__ float g_a(unsigned lo) { return uf(lo << 8); }
__device__ __forceinline__ float g_b(unsigned lo, unsigned hi) { return uf(__builtin_amdgcn_perm(hi, lo, 0x0504030cu)); }
__device__ __forceinline__ bool tag_ok(v4u r, unsigned tagw) { return (((r.y ^ tagw) | (r.w ^ tagw)) >> 16) == 0u; }

template <int CTRL> __device__ __forceinline__ float dppm(float v) {
    return __builtin_bit_cast(float, __builtin_amdgcn_mov_dpp(__builtin_bit_cast(int, v), CTRL, 0xf, 0xf, true));
}
__device__ __forceinline__ float row_sum(float v) {
    v += dppm<0xB1>(v); v += dppm<0x4E>(v); v += dppm<0x141>(v); v += dppm<0x128>(v);
    return v;
}
__device__ __forceinline__ float half_sum(float x) {
    auto t = __builtin_amdgcn_permlane32_swap(fu(x), fu(x), false, false);
    return uf(t[0]) + uf(t[1]);
}
__device__ __forceinline__ float xrow_max(float x) {
    auto s = __builtin_amdgcn_permlane16_swap(fu(x), fu(x), false, false);
    x = fmx(uf(s[0]), uf(s[1]));
    auto t = __builtin_amdgcn_permlane32_swap(fu(x), fu(x), false, false);
    return fmx(uf(t[0]), uf(t[1]));
}
__device__ __forceinline__ float xrow_sum(float x) {
    auto s = __builtin_amdgcn_permlane16_swap(fu(x), fu(x), false, false);
    return half_sum(uf(s[0]) + uf(s[1]));
}
__device__ __forceinline__ float wave_sum(float v) { return xrow_sum(row_sum(v)); }
__device__ __forceinline__ float wave_max(float x) {
    x = fmx(x, dppm<0xB1>(x)); x = fmx(x, dppm<0x4E>(x)); x = fmx(x, dppm<0x141>(x)); x = fmx(x, dppm<0x128>(x));
    return xrow_max(x);
}
__device__ __forceinline__ f32x4 mfma(v4u a, v4u b, f32x4 c) {
    return __builtin_amdgcn_mfma_f32_16x16x32_bf16(__builtin_bit_cast(bf16x8, a), __builtin_bit_cast(bf16x8, b), c, 0, 0, 0);
}
__device__ __forceinline__ f32x4 mfma16(v2u a, v2u b, f32x4 c) {
    return __builtin_amdgcn_mfma_f32_16x16x16bf16_1k(__builtin_bit_cast(s16x4, a), __builtin_bit_cast(s16x4, b), c, 0, 0, 0);
}
__device__ __forceinline__ v2u trread(LAS unsigned char* L, unsigned off) {
    return __builtin_bit_cast(v2u, __builtin_amdgcn_ds_read_tr16_b64_v4i16((LAS s16x4*)(L + off)));
}
__device__ __forceinline__ v4u trfrag(LAS unsigned char* L, unsigned off, unsigned toff) {
    const v2u a = trread(L, off), b = trread(L, off + toff);
    return v4u{a.x, a.y, b.x, b.y};
}
__device__ __forceinline__ v4u xfrag(LAS unsigned char* L, unsigned off) {
    const v2u a = *(LAS v2u*)(L + off), b = *(LAS v2u*)(L + off + 32);
    return v4u{a.x, a.y, b.x, b.y};
}
#define CBAR() asm volatile("" ::: "memory")
#define LGKM0() asm volatile("s_waitcnt lgkmcnt(0)" ::: "memory")
#define BARRIER() asm volatile("s_waitcnt lgkmcnt(0)\n\ts_barrier" ::: "memory")
#define PIN(v) asm volatile("" : "+v"(v))
__device__ __forceinline__ void wait_ge_at(unsigned addr, unsigned want) {
    unsigned v, sv, n = LDS_SPIN;
    asm volatile("s_branch 1f\n"
                 "0:\n\t"
                 "s_sleep 1\n"
                 "1:\n\t"
                 "ds_read_b32 %0, %3\n\t"
                 "s_sub_u32 %2, %2, 1\n\t"
                 "s_cbranch_scc1 2f\n\t"
                 "s_waitcnt lgkmcnt(0)\n\t"
                 "v_readfirstlane_b32 %1, %0\n\t"
                 "s_cmp_lt_u32 %1, %4\n\t"
                 "s_cbranch_scc1 0b\n"
                 "2:\n\t"
                 "s_waitcnt lgkmcnt(0)"
                 : "=&v"(v), "=&s"(sv), "+s"(n) : "v"(addr), "s"(want) : "memory", "scc");
}
#define wait_ge(ptr, want) wait_ge_at(L_FLAG + 4u * (unsigned)((ptr) - FL), (want))
__device__ __forceinline__ void wait_until(unsigned t) {
#pragma nounroll
    for (int s = 0; s < 1000 && (int)((unsigned)__builtin_amdgcn_s_memrealtime() - t) < 0; ++s) __builtin_amdgcn_s_sleep(1);
}
#define SIGNAL(flag) asm volatile("s_waitcnt lgkmcnt(0)\n\tds_add_u32 %0, %1 offset:%2" ::"v"(lane == 0 ? L_FLAG : L_JUNK + 4u * lane), "v"(1u), "i"(4 * (flag)) : "memory")
__device__ __forceinline__ v4i mkrsrc(const void* p, unsigned bytes) {
    const uint64_t a = (uint64_t)p;
    return v4i{(int)(unsigned)a, (int)((unsigned)(a >> 32) & 0xffffu), (int)bytes, 0x00020000};
}
#define VMWAIT(n) asm volatile("s_waitcnt vmcnt(%0)" ::"i"(n) : "memory")
#define LD(voff, soff) __builtin_amdgcn_raw_buffer_load_b128(wsr, (voff), (soff), AUX_SC1)
#define ST(v, voff) __builtin_amdgcn_raw_buffer_store_b128((v), wsr, (voff), 0, AUX_SC1)

#define STAMP(i) do { } while (0)
#define STAMP_FLUSH() do { } while (0)
#define STAMP_OUT() do { } while (0)

template <int DEP>
__device__ __forceinline__ void dma4(v4i rs, v4u v, unsigned& soff, unsigned sstep, unsigned lds) {
    asm volatile("s_mov_b32 m0, %[lds]\n\t"
                 "s_nop 0\n\t"
                 "buffer_load_dwordx4 %[v0], %[rs], %[so] offen nt lds\n\t"
                 "s_add_u32 m0, m0, 0x400\n\t"
                 "s_add_u32 %[so], %[so], %[st]\n\t"
                 "buffer_load_dwordx4 %[v1], %[rs], %[so] offen nt lds\n\t"
                 "s_add_u32 m0, m0, 0x400\n\t"
                 "s_add_u32 %[so], %[so], %[st]\n\t"
                 "s_waitcnt vmcnt(%[dep])\n\t"
                 "buffer_load_dwordx4 %[v2], %[rs], %[so] offen nt lds\n\t"
                 "s_add_u32 m0, m0, 0x400\n\t"
                 "s_add_u32 %[so], %[so], %[st]\n\t"
                 "buffer_load_dwordx4 %[v3], %[rs], %[so] offen nt lds\n\t"
                 "s_add_u32 %[so], %[so], %[st]\n\t"
                 "s_waitcnt vmcnt(%[dep])"
                 : [so] "+s"(soff)
                 : [lds] "s"(lds), [v0] "v"(v.x), [v1] "v"(v.y), [v2] "v"(v.z), [v3] "v"(v.w), [rs] "s"(rs), [st] "s"(sstep), [dep] "i"(DEP)
                 : "memory", "scc");
}
template <int DEP>
__device__ __forceinline__ void stream(v4i rs, v4u va, v4u vb, unsigned soff, unsigned sstep, unsigned lds, int ng, int& prog, unsigned progaddr) {
    const unsigned end = lds + ng * 4096;
#pragma nounroll
    do {
        dma4<DEP>(rs, va, soff, sstep, lds);
        prog += 4;
        asm volatile("ds_max_i32 %0, %1" ::"v"(progaddr), "v"(prog - DEP) : "memory");
        dma4<DEP>(rs, vb, soff, sstep, lds + 4096);
        prog += 4; lds += 8192;
        asm volatile("ds_max_i32 %0, %1" ::"v"(progaddr), "v"(prog - DEP) : "memory");
    } while (lds != end);
}

__global__ void __launch_bounds__(1024, 4)
fused_decode(const u16* __restrict__ wq, const u16* __restrict__ x, unsigned char* ws, const u16* __restrict__ kc, const u16* __restrict__ vc,
             const u16* __restrict__ wo, const u16* __restrict__ wg, const u16* __restrict__ wu, const u16* __restrict__ wd, u16* out) {
    extern __shared__ __attribute__((aligned(16))) unsigned char smem[];
    LAS unsigned char* L = (LAS unsigned char*)smem;
    volatile LAS unsigned* FL = (volatile LAS unsigned*)(L + L_FLAG);
    const int tid = threadIdx.x, lane = tid & 63, wave = __builtin_amdgcn_readfirstlane(tid >> 6), b = blockIdx.x;
    STAMP(0);
    FL[lane & 15] = 0u;
    v4u xa, xb;
    v2u xs, xg;
    unsigned ep;
    {
        const int rl = lane >> 3, p = lane & 7;
        const unsigned v0 = NOSWZ ? rl * 8192 + p * 16 : rl * 8192 + (((p >> 1) ^ ((rl >> 1) & 3)) << 5) + (p & 1) * 16;
        unsigned so = (unsigned)(b >> 6) * 256 * 8192 + (b & 63) * 128;
        const v4i rs = mkrsrc(wq, DM * 4096 * 2);
        const unsigned l16 = lane * 16, zero = 0;
        const unsigned os = (b >> 6) * 512 + lane * 8, og = ((b & 7) * 2 + (b >> 7)) * 128 + (lane & 15) * 8;
        uint64_t pc;
        unsigned coff = (b >> 3) * 1024 + l16;
        asm volatile("s_cmp_lg_u32 %[wave], 0\n\t"
                     "s_cbranch_scc1 1f\n\t"
                     "s_mov_b32 m0, %[lds]\n"
                     "0:\n\t"
                     "s_nop 0\n\t"
                     "buffer_load_dwordx4 %[v0], %[rs], %[so] offen nt lds\n\t"
                     "s_add_u32 m0, m0, 0x400\n\t"
                     "s_add_u32 %[so], %[so], 0x10000\n\t"
                     "buffer_load_dwordx4 %[v0], %[rs], %[so] offen nt lds\n\t"
                     "s_add_u32 m0, m0, 0x400\n\t"
                     "s_add_u32 %[so], %[so], 0x10000\n\t"
                     "buffer_load_dwordx4 %[v0], %[rs], %[so] offen nt lds\n\t"
                     "s_add_u32 m0, m0, 0x400\n\t"
                     "s_add_u32 %[so], %[so], 0x10000\n\t"
                     "buffer_load_dwordx4 %[v0], %[rs], %[so] offen nt lds\n\t"
                     "s_add_u32 m0, m0, 0x400\n\t"
                     "s_add_u32 %[so], %[so], 0x10000\n\t"
                     "s_cmp_lg_u32 m0, %[lend]\n\t"
                     "s_cbranch_scc1 0b\n\t"
                     "s_branch 3f\n"
                     "1:\n\t"
                     "s_cmp_lg_u32 %[wave], 1\n\t"
                     "s_cbranch_scc1 2f\n\t"
                     "global_load_dwordx4 %[xa], %[l16], %[x]\n\t"
                     "global_load_dwordx4 %[xb], %[l16], %[x] offset:1024\n\t"
                     "global_load_dwordx2 %[xs], %[os], %[x]\n\t"
                     "global_load_dwordx2 %[xg], %[og], %[x]\n\t"
                     "global_load_dword %[ep], %[zero], %[ws] sc1\n\t"
                     "s_branch 3f\n"
                     "2:\n\t"
                     "s_cmp_lg_u32 %[wave], %[pfw]\n\t"
                     "s_cbranch_scc1 3f\n\t"
                     "s_mov_b32 m0, %[junk]\n\t"
                     ".subsection 1\n"
                     "5:\n\t"
                     ".subsection 0\n\t"
                     "v_min_u32 %[coff], 5b-4f-16, %[coff]\n\t"
                     "s_getpc_b64 %[pc]\n"
                     "4:\n\t"
                     "global_load_lds_dwordx4 %[coff], %[pc]\n"
                     "3:\n\t"
                     "s_waitcnt lgkmcnt(0)\n\t"
                     "s_barrier"
                     : [so] "+s"(so), [xa] "=&v"(xa), [xb] "=&v"(xb), [xs] "=&v"(xs), [xg] "=&v"(xg), [ep] "=&v"(ep), [pc] "=&s"(pc), [coff] "+v"(coff)
                     : [wave] "s"(wave), [pfw] "s"(PFMODE == 0 ? 2 : PFMODE == 1 ? (b < 128 ? 2 : 99) : 99), [lds] "i"(L_Y), [lend] "i"(L_Y + 24576), [junk] "i"(L_JUNK), [v0] "v"(v0), [rs] "s"(rs), [l16] "v"(l16), [zero] "v"(zero), [os] "v"(os), [og] "v"(og), [x] "s"(x), [ws] "s"(ws)
                       , "s"(kc), "s"(vc), "s"(wo), "s"(wg), "s"(wu), "s"(wd), "s"(out)
                     : "memory", "scc");
        const u16 *p1 = (b & 8) ? kc : wg, *p2 = (b & 8) ? vc : wu, *p3 = (b & 8) ? wo : wd;
        if (wave == 0) {
            const int xc = b & 7, bi = b >> 3, idx = xc * 16 + (bi >> 1), ks = idx >> 1, ct2 = idx & 1;
            const int lj = lane & 15, lg = lane >> 4;
            const unsigned pa = lane == 0 ? L_FLAG + 4 * F_PROG : L_JUNK + 4 * lane;
            int cnt = 24;
            PIN(cnt);

            stream<28>(rs, v4u{v0, v0, v0, v0}, v4u{v0, v0, v0, v0}, so, 65536, L_Y + 24576, 2, cnt, pa);
            STAMP(1);
            v4u va, vb;
#pragma unroll
            for (int i = 0; i < 4; ++i) { va[i] = NOSWZ ? l16 : l16 ^ (i << 5); vb[i] = NOSWZ ? l16 : l16 ^ ((i + 4) << 5); }
            if (bi & 1) {
                const size_t kvoff = ((size_t)(xc >> 1) * SK + (SK - WIN) + (idx & 31) * 128) * 128;
                const unsigned k0 = lg * 256 + ((lj ^ lg) << 4);
                const v4u kv = NOSWZ ? v4u{l16, l16, l16, l16} : v4u{k0, k0 ^ 0x40u, k0 ^ 0x80u, k0 ^ 0xC0u};
                stream<D_A>(mkrsrc(p1 + kvoff, 32768), kv, kv, 0, 1024, L_X, 8, cnt, pa);
                const unsigned u0 = lg * 256 + (((lj >> 1) ^ lg) << 5) + (lj & 1) * 16;
                const v4u vv = NOSWZ ? v4u{l16, l16, l16, l16} : v4u{u0, u0 ^ 0x80u, u0, u0 ^ 0x80u};
                stream<D_A>(mkrsrc(p2 + kvoff, 32768), vv, vv, 0, 1024, L_X + 32768, 8, cnt, pa);
                STAMP(2);
                const v4i wr = mkrsrc(p3, 4096 * DM * 2);
                const unsigned so2 = (unsigned)((ks >> 2) * 256 + (ks & 3) * 32) * 2048 + ct2 * 1024;
                VMWAIT(0);
                STAMP(3);
                BARRIER();
                if (W_PRE) stream<32>(wr, va, vb, so2, 2048, L_Y, W_PRE, cnt, pa);
                BARRIER();
                if (W_PRE < 8) stream<D_W>(wr, va, vb, so2 + W_PRE * 4 * 2048, 2048, L_Y + W_PRE * 4096, 8 - W_PRE, cnt, pa);
                stream<D_W2>(wr, va, vb, so2 + 128 * 2048, 2048, L_Y + 32768, 8, cnt, pa);
            } else {
                v4u ga, gb;
#pragma unroll
                for (int i = 0; i < 4; ++i) { ga[i] = va[i] < 896 ? va[i] : 0x40000000u; gb[i] = vb[i] < 896 ? vb[i] : 0x40000000u; }
                const unsigned so2 = (unsigned)(idx >> 3) * 64 * 7168 + (idx & 7) * 896;
                stream<D_B>(mkrsrc(p1, DM * FF * 2), ga, gb, so2, 7168, L_X, 16, cnt, pa);
                STAMP(2);
                wait_ge(FL + F_WQDONE, 2);
                stream<D_B>(mkrsrc(p2, DM * FF * 2), ga, gb, so2, 7168, L_Y, 16, cnt, pa);
                STAMP(3);
                wait_ge(FL + F_GDONE, 14);
                const v4i dr = mkrsrc(p3, FF * DM * 2);
                const unsigned so3 = (unsigned)ks * 56 * 2048 + ct2 * 1024;
                stream<D_D>(dr, va, vb, so3, 2048, L_X, 14, cnt, pa);
                VMWAIT(24);
                asm volatile("ds_max_i32 %0, %1" ::"v"(pa), "v"(cnt - 24) : "memory");
            }
            STAMP(4);
            VMWAIT(0);
            STAMP(5);
            asm volatile("s_barrier" ::: "memory");
            STAMP_FLUSH();
            return;
        }
    }
    const int xc = b & 7, bi = b >> 3;
    const bool att = bi & 1;
    const int idx = xc * 16 + (bi >> 1);
    const int ks = idx >> 1, ct2 = idx & 1;
    const int lj = lane & 15, lg = lane >> 4;
    const int q4 = lj >> 2, p4 = lane & 3;
    const unsigned key = (unsigned)(q4 | ((lg & 1) << 2));
    const __amdgpu_buffer_rsrc_t wsr = __builtin_amdgcn_make_buffer_rsrc((void*)ws, 0, NOHO ? 0 : (int)WS_END, 0x00020000);

    if (wave == 1) {
        asm volatile("s_waitcnt vmcnt(0)" : "+v"(xa), "+v"(xb), "+v"(xs), "+v"(xg), "+v"(ep)::"memory");
        const float c0 = bflo(__builtin_amdgcn_readfirstlane(xa.x));
        f32x2 s1 = {0.f, 0.f}, s2 = {0.f, 0.f};
#pragma unroll
        for (int i = 0; i < 4; ++i) {
            const f32x2 da = f32x2{bflo(xa[i]), bfhi(xa[i])} - c0, db = f32x2{bflo(xb[i]), bfhi(xb[i])} - c0;
            s1 += da + db;
            s2 += da * da; s2 += db * db;
        }
        const float md = wave_sum(s1[0] + s1[1]) * (1.0f / DM), e2 = wave_sum(s2[0] + s2[1]) * (1.0f / DM);
        const float var = fmx(fmaf(-md, md, e2), 0.f);
        const float mb = rbf(c0 + md), den = rbf(__builtin_amdgcn_sqrtf(rbf(rbf(var) + 1e-5f)));
        const float rden = __builtin_amdgcn_rcpf(den);
        v2u oq, og2;
#pragma unroll
        for (int i = 0; i < 2; ++i) {
            unsigned dq = pkbf(bflo(xs[i]) - mb, bfhi(xs[i]) - mb), dg = pkbf(bflo(xg[i]) - mb, bfhi(xg[i]) - mb);
            PIN(dq); PIN(dg);
            oq[i] = pkbf(bflo(dq) * rden, bfhi(dq) * rden);
            og2[i] = pkbf(bflo(dg) * rden, bfhi(dg) * rden);
        }
        *(LAS v2u*)(L + L_XN + lane * 8) = oq;
        *(LAS v2u*)(L + L_XN + 512 + lane * 8) = og2;
        const unsigned tag = ep + 1u, tagw = tag << 16;
        FL[F_TAG] = tag;
        SIGNAL(F_XN);
        STAMP(1);
        const int own = xc * 32 + bi, col = own * 4 + ((lg & 1) << 1) + (lg >> 1);
        unsigned o_s3 = lane * 4096 + own * 16;
        const float xres = bflo(x[col]);
        PIN(o_s3);
        u16* outp = out + col;
        asm volatile("" : "+v"(outp));
        if (att) {
            unsigned a_o = L_O + (lj & 4) * 16 + (lj >> 3) * 32 + (lj & 3) * 8 + ((lg & 1) << 2) + (lg & 2);
            PIN(a_o);
            BARRIER();
            BARRIER();
            __builtin_amdgcn_s_sleep(S2_SLEEP);
            STAMP(2);
            const unsigned o_ml = ((ks >> 4) * 32 + (lane & 31)) * 128 + (((ks >> 2) & 3) * 2 + (lane >> 5)) * 16, o_r = ks * 8192 + lane * 16;
            v4u r[8], mlv = LD(o_ml, WS_S2M);
#pragma unroll
            for (int i = 0; i < 8; ++i) r[i] = LD(o_r, WS_S2O + i * 1024);
#pragma nounroll
            for (unsigned sp = 0; sp < GLB_SPIN; ++sp) {
                unsigned bad = (mlv.y ^ tag) | (mlv.w ^ tag), bad2 = 0;
#pragma unroll
                for (int i = 0; i < 8; ++i) bad2 |= (r[i].y ^ tagw) | (r[i].w ^ tagw);
                if (__builtin_expect(__all((bad | (bad2 >> 16)) == 0u), 1)) break;
                CBAR();
                if (bad) mlv = LD(o_ml, WS_S2M);
#pragma unroll
                for (int i = 0; i < 8; ++i) if (!tag_ok(r[i], tagw)) r[i] = LD(o_r, WS_S2O + i * 1024);
            }
            STAMP(3);
            float Mx = uf(mlv.x);
            Mx = fmx(Mx, dppm<0xB1>(Mx)); Mx = fmx(Mx, dppm<0x4E>(Mx)); Mx = fmx(Mx, dppm<0x141>(Mx)); Mx = fmx(Mx, dppm<0x128>(Mx));
            { auto t = __builtin_amdgcn_permlane16_swap(fu(Mx), fu(Mx), false, false); Mx = fmx(uf(t[0]), uf(t[1])); }
            const float wgt = __builtin_amdgcn_exp2f(uf(mlv.x) - Mx);
            float w[8];
#pragma unroll
            for (int i = 0; i < 8; ++i) w[i] = uf(__builtin_amdgcn_ds_bpermute((lg + (lj & 4) * 8) * 4 + 16 * i, fu(wgt)));
            float Ls = row_sum(wgt * uf(mlv.z));
            { auto t = __builtin_amdgcn_permlane16_swap(fu(Ls), fu(Ls), false, false); Ls = uf(t[0]) + uf(t[1]); }
            Ls = __builtin_amdgcn_rcpf(Ls);
            const float rl = (lj & 4) ? uf(__builtin_amdgcn_readlane(fu(Ls), 32)) : uf(__builtin_amdgcn_readlane(fu(Ls), 0));
            f32x2 a01 = {0.f, 0.f}, a23 = {0.f, 0.f};
#pragma unroll
            for (int i = 0; i < 8; ++i) {
                a01 += w[i] * f32x2{g_a(r[i].x), g_b(r[i].x, r[i].y)};
                a23 += w[i] * f32x2{g_a(r[i].z), g_b(r[i].z, r[i].w)};
            }
            auto t01 = __builtin_amdgcn_permlane32_swap(fu(a01[0]), fu(a01[1]), false, false);
            auto t23 = __builtin_amdgcn_permlane32_swap(fu(a23[0]), fu(a23[1]), false, false);
            auto tt = __builtin_amdgcn_permlane16_swap(fu(uf(t01[0]) + uf(t01[1])), fu(uf(t23[0]) + uf(t23[1])), false, false);
            *(LAS __bf16*)(L + a_o) = (__bf16)((uf(tt[0]) + uf(tt[1])) * rl);
        } else {
            wait_ge(FL + F_PROG, 160 + SH_AT);
            if (SH_SLEEP) __builtin_amdgcn_s_sleep(SH_SLEEP);
            STAMP(2);
            const int un = lane & 31;
            const unsigned off = ((lane >> 5) * FF + ks * 56 + (un < 28 ? un : 27) * 2) * 8;
            v4u r[8];
#pragma unroll
            for (int i = 0; i < 8; ++i) r[i] = LD(off, WS_SH + i * 2 * FF * 8);
            f32x2 c0, c1;
#pragma nounroll
            for (unsigned sp = 0; sp < GLB_SPIN; ++sp) {
                unsigned bad = 0;
                c0 = f32x2{0.f, 0.f}; c1 = f32x2{0.f, 0.f};
#pragma unroll
                for (int i = 0; i < 8; ++i) {
                    bad |= (r[i].y ^ tagw) | (r[i].w ^ tagw);
                    c0 += f32x2{g_a(r[i].x), g_b(r[i].x, r[i].y)};
                    c1 += f32x2{g_a(r[i].z), g_b(r[i].z, r[i].w)};
                }
                if (__builtin_expect(__all((bad >> 16) == 0u), 1)) break;
                CBAR();
#pragma unroll
                for (int i = 0; i < 8; ++i) if (!tag_ok(r[i], tagw)) r[i] = LD(off, WS_SH + i * 2 * FF * 8);
            }
            STAMP(3);
            auto tg = __builtin_amdgcn_permlane32_swap(fu(c0[0]), fu(c1[0]), false, false);
            auto tu = __builtin_amdgcn_permlane32_swap(fu(c0[1]), fu(c1[1]), false, false);
            const unsigned gu = pkbf(uf(tg[0]) + uf(tg[1]), uf(tu[0]) + uf(tu[1]));
            const float g = bflo(gu);
            const float hv = rbf(g * __builtin_amdgcn_rcpf(1.0f + __builtin_amdgcn_exp2f(-g * LOG2E))) * bfhi(gu);
            *(LAS __bf16*)(L + L_O + un * 4 + (lane >> 5) * 2) = (__bf16)(un < 28 ? hv : 0.f);
        }
        STAMP(4);
        BARRIER();
        {
        __builtin_amdgcn_s_sleep(S3_SLEEP);
        v4u r0 = LD(o_s3, WS_S3), r1 = LD(o_s3, WS_S3 + 64 * 4096);
#pragma nounroll
        for (unsigned sp = 0; sp < GLB_SPIN; ++sp) {
            const bool ok0 = tag_ok(r0, tagw), ok1 = tag_ok(r1, tagw);
            if (__all(ok0 & ok1)) break;
            CBAR();
            if (!ok0) r0 = LD(o_s3, WS_S3);
            if (!ok1) r1 = LD(o_s3, WS_S3 + 64 * 4096);
        }
        STAMP(5);
        const float s0 = g_a(r0.x) + g_a(r1.x), s1 = g_b(r0.x, r0.y) + g_b(r1.x, r1.y);
        const float s2 = g_a(r0.z) + g_a(r1.z), s3 = g_b(r0.z, r0.w) + g_b(r1.z, r1.w);
        auto t01 = __builtin_amdgcn_permlane32_swap(fu(s0), fu(s1), false, false);
        auto t23 = __builtin_amdgcn_permlane32_swap(fu(s2), fu(s3), false, false);
        auto t = __builtin_amdgcn_permlane16_swap(fu(uf(t01[0]) + uf(t01[1])), fu(uf(t23[0]) + uf(t23[1])), false, false);
        const float tot = row_sum(uf(t[0]) + uf(t[1])) + xres;
        if (lj == 0) {
            *(GAS __bf16*)outp = (__bf16)tot;
            if (idx == 0 && (S3W == 32 || !att) && lane == 0) __hip_atomic_store((GAS unsigned*)(ws + WS_EPOCH), tag, __ATOMIC_RELAXED, __HIP_MEMORY_SCOPE_AGENT);
        }
        }
        STAMP(6);
        STAMP_FLUSH();
        STAMP_OUT();
        return;
    }
    if (att && wave < 6) { STAMP_FLUSH(); return; }
    wait_ge(FL + F_XN, 1);
    const unsigned tag = FL[F_TAG], tagw = tag << 16;
    if ((wave & 14) == 6) {
        const int c = wave & 1;
        const unsigned row = 4 * lg + q4;
        const unsigned a0 = L_Y + row * 128 + (((2 * c) ^ ((row >> 1) & 3)) << 5) + p4 * 8, a1 = a0 ^ 32u;
        unsigned dst = ((b >> 6) * 4096 + (b & 63) * 64 + c * 32 + lj * 16 + lg * 4) * 4 + WS_S1;
        PIN(dst);
        f32x4 acc0 = {0.f, 0.f, 0.f, 0.f}, acc1 = {0.f, 0.f, 0.f, 0.f};
        v4u xq[8], af[6], ag[6];
#pragma unroll
        for (int s = 0; s < 8; ++s) xq[s] = xfrag(L, L_XN + lg * 8 + s * 64);
        wait_ge(FL + F_PROG, 24);
#pragma unroll
        for (int s = 0; s < 6; ++s) { af[s] = trfrag(L, a0 + s * 4096, 2048); ag[s] = trfrag(L, a1 + s * 4096, 2048); }
        __builtin_amdgcn_sched_barrier(0);
#pragma unroll
        for (int s = 0; s < 6; ++s) { acc0 = mfma(af[s], xq[s], acc0); acc1 = mfma(ag[s], xq[s], acc1); }
        __builtin_amdgcn_sched_barrier(0);
        wait_ge(FL + F_PROG, 32);
#pragma unroll
        for (int s = 0; s < 2; ++s) { af[s] = trfrag(L, a0 + (6 + s) * 4096, 2048); ag[s] = trfrag(L, a1 + (6 + s) * 4096, 2048); }
        __builtin_amdgcn_sched_barrier(0);
#pragma unroll
        for (int s = 0; s < 2; ++s) { acc0 = mfma(af[s], xq[6 + s], acc0); acc1 = mfma(ag[s], xq[6 + s], acc1); }
        const f32x4 acc = lj == 0 ? acc0 : acc1;
        if (lj < 2) ST(pack4(acc[0], acc[1], acc[2], acc[3], tagw), dst);
        SIGNAL(F_WQDONE);
        STAMP(1);
        if (att) { STAMP_FLUSH(); return; }
    }
    const unsigned rbase = (4 * lg + q4) * 1024 + p4 * 8;
    if (att) {
        const int aw = wave - 8, gkv = xc >> 1, split = idx & 31;
        unsigned a_q = L_Q + (lj & 7) * 288 + lg * 16, a_k = L_X + (16 * aw + lj) * 256, a_v = L_X + 32768 + (4 * lg + q4) * 256 + ((aw ^ key) << 5) + p4 * 8;
        unsigned a_p = L_P + lane * 8, a_ml = L_ML + (lj & 7) * 80;
        unsigned dst = (((gkv * 4 + (lj >> 1)) * 4 + (aw >> 1)) * 32 + split) * 256 + (aw & 1) * 128 + (lj & 1) * 64 + lg * 16 + WS_S2O;
        unsigned k0 = a_k + ((lg ^ lj) << 4), k1 = a_k + (((4 + lg) ^ lj) << 4), k2 = a_k + (((8 + lg) ^ lj) << 4), k3 = a_k + (((12 + lg) ^ lj) << 4);
        PIN(a_q); PIN(a_v); PIN(a_p); PIN(a_ml); PIN(dst);
        wait_ge(FL + F_PROG, S1_AT);
        STAMP(1);
        v4u kf[4];
        kf[0] = *(LAS v4u*)(L + k0); kf[1] = *(LAS v4u*)(L + k1); kf[2] = *(LAS v4u*)(L + k2); kf[3] = *(LAS v4u*)(L + k3);
        {
            v4u r0, r1;
#pragma nounroll
            for (unsigned sp = 0; sp < GLB_SPIN; ++sp) {
                r0 = LD((lane >> 5) * 16384 + gkv * 4096 + aw * 512 + (lane & 31) * 16, WS_S1);
                r1 = LD((lane >> 5) * 16384 + gkv * 4096 + aw * 512 + (lane & 31) * 16, WS_S1 + 32768);
                if (__all(tag_ok(r0, tagw) & tag_ok(r1, tagw))) break;
                CBAR();
            }
            STAMP(2);
            const float q0 = half_sum(g_a(r0.x) + g_a(r1.x)), q1 = half_sum(g_b(r0.x, r0.y) + g_b(r1.x, r1.y));
            const float q2 = half_sum(g_a(r0.z) + g_a(r1.z)), q3 = half_sum(g_b(r0.z, r0.w) + g_b(r1.z, r1.w));
            if (lane < 32) *(LAS v2u*)(L + L_Q + aw * 288 + lane * 8) = v2u{pkbf(q0, q1), pkbf(q2, q3)};
        }
        BARRIER();
        STAMP(3);
        v4u qf[4];
#pragma unroll
        for (int s = 0; s < 4; ++s) qf[s] = *(LAS v4u*)(L + a_q + s * 64);
        __builtin_amdgcn_sched_barrier(0);
        f32x4 sc = {0.f, 0.f, 0.f, 0.f};
#pragma unroll
        for (int s = 0; s < 4; ++s) sc = mfma(kf[s], qf[s], sc);
        __builtin_amdgcn_sched_barrier(0);
        v2u vf[8];
#pragma unroll
        for (int t = 0; t < 8; ++t) vf[t] = trread(L, a_v + t * 4096);
        __builtin_amdgcn_sched_barrier(0);
        {
            const unsigned r01 = pkbf(sc[0], sc[1]), r23 = pkbf(sc[2], sc[3]);
            const float SC = 0.08838834764831845f;
            const unsigned s01 = pkbf(bflo(r01) * SC, bfhi(r01) * SC), s23 = pkbf(bflo(r23) * SC, bfhi(r23) * SC);
            sc[0] = bflo(s01); sc[1] = bfhi(s01); sc[2] = bflo(s23); sc[3] = bfhi(s23);
        }
        const float m = xrow_max(fmx(fmx(sc[0], sc[1]), fmx(sc[2], sc[3]))) * LOG2E;
#pragma unroll
        for (int r = 0; r < 4; ++r) sc[r] = __builtin_amdgcn_exp2f(fmaf(sc[r], LOG2E, -m));
        *(LAS v2u*)(L + a_p + aw * 512) = v2u{pkbf(sc[0], sc[1]), pkbf(sc[2], sc[3])};
        const float l = xrow_sum((sc[0] + sc[1]) + (sc[2] + sc[3]));
        if (lane < 8) *(LAS v2u*)(L + a_ml + aw * 8) = v2u{fu(m), fu(l)};
        BARRIER();
        STAMP(4);
        v2u pf[8];
        f32x4 ml[4];
#pragma unroll
        for (int t = 0; t < 4; ++t) ml[t] = *(LAS f32x4*)(L + a_ml + t * 16);
#pragma unroll
        for (int t = 0; t < 8; ++t) pf[t] = *(LAS v2u*)(L + a_p + t * 512);
        __builtin_amdgcn_sched_barrier(0);
        f32x4 ot[8];
#pragma unroll
        for (int t = 0; t < 8; ++t) ot[t] = mfma16(vf[t], pf[t], f32x4{0.f, 0.f, 0.f, 0.f});
        const float M = fmx(fmx(fmx(ml[0][0], ml[0][2]), fmx(ml[1][0], ml[1][2])), fmx(fmx(ml[2][0], ml[2][2]), fmx(ml[3][0], ml[3][2])));
        f32x4 o = {0.f, 0.f, 0.f, 0.f};
        float lt = 0.f;
#pragma unroll
        for (int t = 0; t < 8; ++t) {
            const float w = __builtin_amdgcn_exp2f(ml[t >> 1][(t & 1) * 2] - M);
            lt = fmaf(w, ml[t >> 1][(t & 1) * 2 + 1], lt);
            o += w * ot[t];
        }
        if (lj < 8) ST(pack4(o[0], o[1], o[2], o[3], tagw), dst);
        if (aw == 0 && lane < 8) ST((v4u{fu(M), tag, fu(lt), tag}), (gkv * 32 + split) * 128 + lj * 16 + WS_S2M);
        STAMP(5);
    } else {
        const int gw = wave - 2;
        unsigned a0 = rbase + (((2 * gw) ^ key) << 5), a1 = rbase + (((2 * gw + 1) ^ key) << 5);
        unsigned dst = ((idx >> 3) * FF + (idx & 7) * 448 + gw * 32 + (lj >> 1) * 16 + lg * 4 + (lj & 1) * 2) * 8 + WS_SH;
        PIN(dst);
        v4u xf[2], af[2][2];
#pragma unroll
        for (int s = 0; s < 2; ++s) xf[s] = xfrag(L, L_XN + 512 + lg * 8 + s * 64);
        wait_ge(FL + F_PROG, 96);
#pragma unroll
        for (int s = 0; s < 2; ++s) { af[s][0] = trfrag(L, L_X + a0 + s * 32768, 16384); af[s][1] = trfrag(L, L_X + a1 + s * 32768, 16384); }
        SIGNAL(F_GDONE);
        f32x4 g0 = mfma(af[0][0], xf[0], f32x4{0.f, 0.f, 0.f, 0.f}), g1 = mfma(af[0][1], xf[0], f32x4{0.f, 0.f, 0.f, 0.f});
        g0 = mfma(af[1][0], xf[1], g0); g1 = mfma(af[1][1], xf[1], g1);
        wait_ge(FL + F_PROG, 128);
        f32x4 u0 = mfma(trfrag(L, L_Y + a0, 16384), xf[0], f32x4{0.f, 0.f, 0.f, 0.f}), u1 = mfma(trfrag(L, L_Y + a1, 16384), xf[0], f32x4{0.f, 0.f, 0.f, 0.f});
        const bool hi = lj & 1;
        const f32x4 ga = lj < 2 ? g0 : g1;
        float ge = hi ? ga[2] : ga[0], go = hi ? ga[3] : ga[1];
        PIN(ge); PIN(go);
        wait_ge(FL + F_PROG, 160);
        STAMP(2);
        u0 = mfma(trfrag(L, L_Y + 32768 + a0, 16384), xf[1], u0); u1 = mfma(trfrag(L, L_Y + 32768 + a1, 16384), xf[1], u1);
        const f32x4 ua = lj < 2 ? u0 : u1;
        if (lj < 4) ST(pack4(ge, hi ? ua[2] : ua[0], go, hi ? ua[3] : ua[1], tagw), dst);
        STAMP(3);
        if (wave < 8) { STAMP_FLUSH(); return; }
    }
    {
        const int fw = wave - 8;
        const unsigned tb = (att ? L_Y : L_X) + rbase;
        unsigned a[4], dst = (((att ? 0 : 64) + ks) * 1024 + ct2 * 512 + fw * 64 + lj * 16 + lg * 4) * 4 + WS_S3, a_h = L_O + lg * 8;
#pragma unroll
        for (int t = 0; t < 4; ++t) a[t] = tb + (((4 * fw + t) ^ key) << 5);
        PIN(dst); PIN(a_h);
        v4u af[4], ag[4];
        wait_ge(FL + F_PROG, att ? 128 : 192);
#pragma unroll
        for (int t = 0; t < 4; ++t) af[t] = trfrag(L, a[t], 16384);
        BARRIER();
        STAMP(6);
        const v4u h0 = xfrag(L, a_h), h1 = xfrag(L, a_h + 64);
#pragma unroll
        for (int t = 0; t < 4; ++t) ag[t] = trfrag(L, a[t] + 32768, 16384);
        __builtin_amdgcn_sched_barrier(0);
        f32x4 c[4];
#pragma unroll
        for (int t = 0; t < 4; ++t) c[t] = mfma(af[t], h0, f32x4{0.f, 0.f, 0.f, 0.f});
        __builtin_amdgcn_sched_barrier(0);
#pragma unroll
        for (int t = 0; t < 4; ++t) c[t] = mfma(ag[t], h1, c[t]);
        f32x4 cs;
#pragma unroll
        for (int k = 0; k < 4; ++k) {
            float lo = (lj & 1) ? c[1][k] : c[0][k], hi = (lj & 1) ? c[3][k] : c[2][k];
            PIN(lo); PIN(hi);
            cs[k] = (lj & 2) ? hi : lo;
        }
        if (lj < 4) ST(pack4(cs[0], cs[1], cs[2], cs[3], tagw), dst);
        STAMP(7);
    }
    STAMP_FLUSH();
}

extern "C" void kernel_launch(void* const* d_in, const int* in_sizes, int n_in, void* d_out, int out_size, void* d_ws, size_t ws_size,
                              hipStream_t stream) {
    (void)in_sizes; (void)n_in; (void)out_size; (void)ws_size;
    (void)hipFuncSetAttribute((const void*)fused_decode, hipFuncAttributeMaxDynamicSharedMemorySize, (int)LDS_BYTES);
    fused_decode<<<256, 1024, LDS_BYTES, stream>>>((const u16*)d_in[3], (const u16*)d_in[0], (unsigned char*)d_ws + WS_OFF, (const u16*)d_in[1],
                                                   (const u16*)d_in[2], (const u16*)d_in[4], (const u16*)d_in[6], (const u16*)d_in[7],
                                                   (const u16*)d_in[8], (u16*)d_out);
}
```

```cpp
#include <hip/hip_runtime.h>
#include <hip/hip_bf16.h>
#include <stdint.h>

#define LAS __attribute__((address_space(3)))
#define GAS __attribute__((address_space(1)))
typedef unsigned short u16;
typedef unsigned v4u __attribute__((ext_vector_type(4)));
typedef unsigned v2u __attribute__((ext_vector_type(2)));
typedef int v4i __attribute__((ext_vector_type(4)));
typedef float f32x4 __attribute__((ext_vector_type(4)));
typedef float f32x2 __attribute__((ext_vector_type(2)));
typedef short s16x4 __attribute__((ext_vector_type(4)));
typedef __bf16 bf16x8 __attribute__((ext_vector_type(8)));
typedef __bf16 bf16x2 __attribute__((ext_vector_type(2)));

constexpr int DM = 1024, SK = 32768, WIN = 4096, FF = 3584;
constexpr unsigned WS_EPOCH = 0;
constexpr unsigned WS_S1 = 4096;
constexpr unsigned WS_SH = WS_S1 + 4 * 4096 * 4;
constexpr unsigned WS_S2O = WS_SH + 16 * 3584 * 8;
constexpr unsigned WS_S2M = WS_S2O + 64 * 32 * 256;
constexpr unsigned WS_S3 = WS_S2M + 32 * 32 * 16;
constexpr unsigned WS_END = WS_S3 + 128 * 1024 * 4;
constexpr unsigned L_X = 0;
constexpr unsigned L_Y = 65536;
constexpr unsigned L_XN = 131072;
constexpr unsigned L_Q = L_XN + 2048;
constexpr unsigned L_P = L_Q + 2304;
constexpr unsigned L_ML = L_P + 4096;
constexpr unsigned L_O = L_ML + 1024;
constexpr unsigned L_FLAG = L_O + 128;
constexpr unsigned L_JUNK = L_FLAG + 64;
constexpr unsigned L_TS = L_JUNK + 1024;
constexpr unsigned LDS_BYTES = L_TS + 1024;
enum { F_PROG = 0, F_XN = 1, F_TAG = 2, F_WQDONE = 3, F_GDONE = 4 };

#define D_A 28
#define D_W 16
#define W_PRE 4
#define W_DELAY 0
#define S3_SLEEP 26
#define D_B 32
#define D_D 28
#define DN_GATE 0
#define DN_PRE 0
#define DN_DELAY 4
#define WS_OFF 0
#define SH_SLEEP 0
#define D_W2 D_W
#define S2_T 0
#define SH_T 0
#define S3_T 0
#define OUT_MODE 0
#define EP_PLAIN 0
#define S1_AT (96 - D_A)
#define SH_AT 32
#define S2_SLEEP 48
constexpr unsigned LDS_SPIN = 1u << 20, GLB_SPIN = 1u << 16;
constexpr int AUX_SC1 = 16;
constexpr float LOG2E = 1.4426950408889634f;

__device__ __forceinline__ float bflo(unsigned u) { return __builtin_bit_cast(float, u << 16); }
__device__ __forceinline__ float bfhi(unsigned u) { return __builtin_bit_cast(float, u & 0xffff0000u); }
__device__ __forceinline__ float rbf(float f) { return (float)(__bf16)f; }
__device__ __forceinline__ unsigned pkbf(float lo, float hi) { bf16x2 v = {(__bf16)lo, (__bf16)hi}; return __builtin_bit_cast(unsigned, v); }
__device__ __forceinline__ unsigned fu(float f) { return __builtin_bit_cast(unsigned, f); }
__device__ __forceinline__ float uf(unsigned u) { return __builtin_bit_cast(float, u); }
__device__ __forceinline__ float fmx(float a, float b) { return __builtin_elementwise_maximum(a, b); }
__device__ __forceinline__ unsigned g_lo(float a, float b) { return __builtin_amdgcn_perm(fu(b), fu(a), 0x05030201u); }
__device__ __forceinline__ unsigned g_hi(float b, unsigned tagw) { return __builtin_amdgcn_perm(tagw, fu(b), 0x07060302u); }
__device__ __forceinline__ v4u pack4(float a, float b, float c, float d, unsigned tagw) { return v4u{g_lo(a, b), g_hi(b, tagw), g_lo(c, d), g_hi(d, tagw)}; }
__device__ __forceinline__ float g_a(unsigned lo) { return uf(lo << 8); }
__device__ __forceinline__ float g_b(unsigned lo, unsigned hi) { return uf(__builtin_amdgcn_perm(hi, lo, 0x0504030cu)); }
__device__ __forceinline__ bool tag_ok(v4u r, unsigned tagw) { return (((r.y ^ tagw) | (r.w ^ tagw)) >> 16) == 0u; }

template <int CTRL> __device__ __forceinline__ float dppm(float v) {
    return __builtin_bit_cast(float, __builtin_amdgcn_mov_dpp(__builtin_bit_cast(int, v), CTRL, 0xf, 0xf, true));
}
__device__ __forceinline__ float row_sum(float v) {
    v += dppm<0xB1>(v); v += dppm<0x4E>(v); v += dppm<0x141>(v); v += dppm<0x128>(v);
    return v;
}
__device__ __forceinline__ float half_sum(float x) {
    auto t = __builtin_amdgcn_permlane32_swap(fu(x), fu(x), false, false);
    return uf(t[0]) + uf(t[1]);
}
__device__ __forceinline__ float xrow_max(float x) {
    auto s = __builtin_amdgcn_permlane16_swap(fu(x), fu(x), false, false);
    x = fmx(uf(s[0]), uf(s[1]));
    auto t = __builtin_amdgcn_permlane32_swap(fu(x), fu(x), false, false);
    return fmx(uf(t[0]), uf(t[1]));
}
__device__ __forceinline__ float xrow_sum(float x) {
    auto s = __builtin_amdgcn_permlane16_swap(fu(x), fu(x), false, false);
    return half_sum(uf(s[0]) + uf(s[1]));
}
__device__ __forceinline__ float wave_sum(float v) { return xrow_sum(row_sum(v)); }
__device__ __forceinline__ float wave_max(float x) {
    x = fmx(x, dppm<0xB1>(x)); x = fmx(x, dppm<0x4E>(x)); x = fmx(x, dppm<0x141>(x)); x = fmx(x, dppm<0x128>(x));
    return xrow_max(x);
}
__device__ __forceinline__ f32x4 mfma(v4u a, v4u b, f32x4 c) {
    return __builtin_amdgcn_mfma_f32_16x16x32_bf16(__builtin_bit_cast(bf16x8, a), __builtin_bit_cast(bf16x8, b), c, 0, 0, 0);
}
__device__ __forceinline__ f32x4 mfma16(v2u a, v2u b, f32x4 c) {
    return __builtin_amdgcn_mfma_f32_16x16x16bf16_1k(__builtin_bit_cast(s16x4, a), __builtin_bit_cast(s16x4, b), c, 0, 0, 0);
}
__device__ __forceinline__ v2u trread(LAS unsigned char* L, unsigned off) {
    return __builtin_bit_cast(v2u, __builtin_amdgcn_ds_read_tr16_b64_v4i16((LAS s16x4*)(L + off)));
}
__device__ __forceinline__ v4u trfrag(LAS unsigned char* L, unsigned off, unsigned toff) {
    const v2u a = trread(L, off), b = trread(L, off + toff);
    return v4u{a.x, a.y, b.x, b.y};
}
__device__ __forceinline__ v4u xfrag(LAS unsigned char* L, unsigned off) {
    const v2u a = *(LAS v2u*)(L + off), b = *(LAS v2u*)(L + off + 32);
    return v4u{a.x, a.y, b.x, b.y};
}
#define CBAR() asm volatile("" ::: "memory")
#define LGKM0() asm volatile("s_waitcnt lgkmcnt(0)" ::: "memory")
#define BARRIER() asm volatile("s_waitcnt lgkmcnt(0)\n\ts_barrier" ::: "memory")
#define PIN(v) asm volatile("" : "+v"(v))
__device__ __forceinline__ void wait_ge(volatile LAS unsigned* w, unsigned want) {
#pragma nounroll
    for (unsigned s = 0; s < LDS_SPIN; ++s) {
        if ((unsigned)__builtin_amdgcn_readfirstlane(*w) >= want) break;
        __builtin_amdgcn_s_sleep(1);
    }
    CBAR();
}
__device__ __forceinline__ void wait_until(unsigned t) {
#pragma nounroll
    for (int s = 0; s < 1000 && (int)((unsigned)__builtin_amdgcn_s_memrealtime() - t) < 0; ++s) __builtin_amdgcn_s_sleep(1);
}
#define SIGNAL(flag) asm volatile("s_waitcnt lgkmcnt(0)\n\tds_add_u32 %0, %1 offset:%2" ::"v"(lane == 0 ? L_FLAG : L_JUNK + 4u * lane), "v"(1u), "i"(4 * (flag)) : "memory")
__device__ __forceinline__ v4i mkrsrc(const void* p, unsigned bytes) {
    const uint64_t a = (uint64_t)p;
    return v4i{(int)(unsigned)a, (int)((unsigned)(a >> 32) & 0xffffu), (int)bytes, 0x00020000};
}
#define VMWAIT(n) asm volatile("s_waitcnt vmcnt(%0)" ::"i"(n) : "memory")
#define LD(voff, soff) __builtin_amdgcn_raw_buffer_load_b128(wsr, (voff), (soff), AUX_SC1)
#define ST(v, voff) __builtin_amdgcn_raw_buffer_store_b128((v), wsr, (voff), 0, AUX_SC1)

#define STAMP(i) do { } while (0)
#define STAMP_FLUSH() do { } while (0)
#define STAMP_OUT() do { } while (0)

template <int DEP>
__device__ __forceinline__ void dma4(v4i rs, v4u v, unsigned& soff, unsigned sstep, unsigned lds) {
    asm volatile("s_mov_b32 m0, %[lds]\n\t"
                 "s_nop 0\n\t"
                 "buffer_load_dwordx4 %[v0], %[rs], %[so] offen nt lds\n\t"
                 "s_add_u32 m0, m0, 0x400\n\t"
                 "s_add_u32 %[so], %[so], %[st]\n\t"
                 "buffer_load_dwordx4 %[v1], %[rs], %[so] offen nt lds\n\t"
                 "s_add_u32 m0, m0, 0x400\n\t"
                 "s_add_u32 %[so], %[so], %[st]\n\t"
                 "s_waitcnt vmcnt(%[dep])\n\t"
                 "buffer_load_dwordx4 %[v2], %[rs], %[so] offen nt lds\n\t"
                 "s_add_u32 m0, m0, 0x400\n\t"
                 "s_add_u32 %[so], %[so], %[st]\n\t"
                 "buffer_load_dwordx4 %[v3], %[rs], %[so] offen nt lds\n\t"
                 "s_add_u32 %[so], %[so], %[st]\n\t"
                 "s_waitcnt vmcnt(%[dep])"
                 : [so] "+s"(soff)
                 : [lds] "s"(lds), [v0] "v"(v.x), [v1] "v"(v.y), [v2] "v"(v.z), [v3] "v"(v.w), [rs] "s"(rs), [st] "s"(sstep), [dep] "i"(DEP)
                 : "memory", "scc");
}
template <int DEP>
__device__ __forceinline__ void stream(v4i rs, v4u va, v4u vb, unsigned soff, unsigned sstep, unsigned lds, int ng, int& prog, unsigned progaddr) {
    const unsigned end = lds + ng * 4096;
#pragma nounroll
    do {
        dma4<DEP>(rs, va, soff, sstep, lds);
        prog += 4;
        asm volatile("ds_max_i32 %0, %1" ::"v"(progaddr), "v"(prog - DEP) : "memory");
        dma4<DEP>(rs, vb, soff, sstep, lds + 4096);
        prog += 4; lds += 8192;
        asm volatile("ds_max_i32 %0, %1" ::"v"(progaddr), "v"(prog - DEP) : "memory");
    } while (lds != end);
}

__global__ void __launch_bounds__(1024, 4)
fused_decode(const u16* __restrict__ wq, const u16* __restrict__ x, unsigned char* ws, const u16* __restrict__ kc, const u16* __restrict__ vc,
             const u16* __restrict__ wo, const u16* __restrict__ wg, const u16* __restrict__ wu, const u16* __restrict__ wd, u16* out) {
    extern __shared__ __attribute__((aligned(16))) unsigned char smem[];
    LAS unsigned char* L = (LAS unsigned char*)smem;
    volatile LAS unsigned* FL = (volatile LAS unsigned*)(L + L_FLAG);
    const int tid = threadIdx.x, lane = tid & 63, wave = __builtin_amdgcn_readfirstlane(tid >> 6), b = blockIdx.x;
    STAMP(0);
    FL[lane & 15] = 0u;
    const unsigned tstart = (unsigned)__builtin_amdgcn_s_memrealtime();
    v4u xa, xb;
    unsigned ep;
    {
        const int rl = lane >> 3, p = lane & 7;
        const unsigned v0 = rl * 8192 + (((p >> 1) ^ ((rl >> 1) & 3)) << 5) + (p & 1) * 16;
        unsigned so = (unsigned)(b >> 6) * 256 * 8192 + (b & 63) * 128;
        const v4i rs = mkrsrc(wq, DM * 4096 * 2);
        const unsigned l16 = lane * 16, zero = 0;
        uint64_t pc;
        unsigned coff = (b >> 3) * 1024 + l16;
        asm volatile("s_cmp_lg_u32 %[wave], 0\n\t"
                     "s_cbranch_scc1 1f\n\t"
                     "s_mov_b32 m0, %[lds]\n"
                     "0:\n\t"
                     "s_nop 0\n\t"
                     "buffer_load_dwordx4 %[v0], %[rs], %[so] offen nt lds\n\t"
                     "s_add_u32 m0, m0, 0x400\n\t"
                     "s_add_u32 %[so], %[so], 0x10000\n\t"
                     "buffer_load_dwordx4 %[v0], %[rs], %[so] offen nt lds\n\t"
                     "s_add_u32 m0, m0, 0x400\n\t"
                     "s_add_u32 %[so], %[so], 0x10000\n\t"
                     "buffer_load_dwordx4 %[v0], %[rs], %[so] offen nt lds\n\t"
                     "s_add_u32 m0, m0, 0x400\n\t"
                     "s_add_u32 %[so], %[so], 0x10000\n\t"
                     "buffer_load_dwordx4 %[v0], %[rs], %[so] offen nt lds\n\t"
                     "s_add_u32 m0, m0, 0x400\n\t"
                     "s_add_u32 %[so], %[so], 0x10000\n\t"
                     "s_cmp_lg_u32 m0, %[lend]\n\t"
                     "s_cbranch_scc1 0b\n\t"
                     "s_branch 3f\n"
                     "1:\n\t"
                     "s_cmp_lg_u32 %[wave], 1\n\t"
                     "s_cbranch_scc1 2f\n\t"
                     "global_load_dwordx4 %[xa], %[l16], %[x]\n\t"
                     "global_load_dwordx4 %[xb], %[l16], %[x] offset:1024\n\t"
                     "global_load_dword %[ep], %[zero], %[ws] sc1\n\t"
                     "s_branch 3f\n"
                     "2:\n\t"
                     "s_cmp_lg_u32 %[wave], 2\n\t"
                     "s_cbranch_scc1 3f\n\t"
                     "s_mov_b32 m0, %[junk]\n\t"
                     ".subsection 1\n"
                     "5:\n\t"
                     ".subsection 0\n\t"
                     "v_min_u32 %[coff], 5b-4f-16, %[coff]\n\t"
                     "s_getpc_b64 %[pc]\n"
                     "4:\n\t"
                     "global_load_lds_dwordx4 %[coff], %[pc]\n"
                     "3:\n\t"
                     "s_waitcnt lgkmcnt(0)\n\t"
                     "s_barrier"
                     : [so] "+s"(so), [xa] "=&v"(xa), [xb] "=&v"(xb), [ep] "=&v"(ep), [pc] "=&s"(pc), [coff] "+v"(coff)
                     : [wave] "s"(wave), [lds] "i"(L_Y), [lend] "i"(L_Y + 24576), [junk] "i"(L_JUNK), [v0] "v"(v0), [rs] "s"(rs), [l16] "v"(l16), [zero] "v"(zero), [x] "s"(x), [ws] "s"(ws),
                       "s"(kc), "s"(vc), "s"(wo), "s"(wg), "s"(wu), "s"(wd), "s"(out)
                     : "memory", "scc");
        const u16 *p1 = (b & 8) ? kc : wg, *p2 = (b & 8) ? vc : wu, *p3 = (b & 8) ? wo : wd;
        if (wave == 0) {
            const int xc = b & 7, bi = b >> 3, idx = xc * 16 + (bi >> 1), ks = idx >> 1, ct2 = idx & 1;
            const int lj = lane & 15, lg = lane >> 4;
            const unsigned pa = lane == 0 ? L_FLAG + 4 * F_PROG : L_JUNK + 4 * lane;
            int cnt = 24;
            PIN(cnt);

            stream<28>(rs, v4u{v0, v0, v0, v0}, v4u{v0, v0, v0, v0}, so, 65536, L_Y + 24576, 2, cnt, pa);
            STAMP(1);
            v4u va, vb;
#pragma unroll
            for (int i = 0; i < 4; ++i) { va[i] = l16 ^ (i << 5); vb[i] = l16 ^ ((i + 4) << 5); }
            if (bi & 1) {
                const size_t kvoff = ((size_t)(xc >> 1) * SK + (SK - WIN) + (idx & 31) * 128) * 128;
                const unsigned k0 = lg * 256 + ((lj ^ lg) << 4);
                const v4u kv = {k0, k0 ^ 0x40u, k0 ^ 0x80u, k0 ^ 0xC0u};
                stream<D_A>(mkrsrc(p1 + kvoff, 32768), kv, kv, 0, 1024, L_X, 8, cnt, pa);
                const unsigned u0 = lg * 256 + (((lj >> 1) ^ lg) << 5) + (lj & 1) * 16;
                const v4u vv = {u0, u0 ^ 0x80u, u0, u0 ^ 0x80u};
                stream<D_A>(mkrsrc(p2 + kvoff, 32768), vv, vv, 0, 1024, L_X + 32768, 8, cnt, pa);
                STAMP(2);
                VMWAIT(0);
                STAMP(3);
                BARRIER();
                const v4i wr = mkrsrc(p3, 4096 * DM * 2);
                const unsigned so2 = (unsigned)((ks >> 2) * 256 + (ks & 3) * 32) * 2048 + ct2 * 1024;
                if (W_PRE) stream<D_W>(wr, va, vb, so2, 2048, L_Y, W_PRE, cnt, pa);
                BARRIER();
                if (W_DELAY) __builtin_amdgcn_s_sleep(W_DELAY);
                stream<D_W>(wr, va, vb, so2 + W_PRE * 4 * 2048, 2048, L_Y + W_PRE * 4096, 8 - W_PRE, cnt, pa);
                stream<D_W2>(wr, va, vb, so2 + 128 * 2048, 2048, L_Y + 32768, 8, cnt, pa);
            } else {
                v4u ga, gb;
#pragma unroll
                for (int i = 0; i < 4; ++i) { ga[i] = va[i] < 896 ? va[i] : 0x40000000u; gb[i] = vb[i] < 896 ? vb[i] : 0x40000000u; }
                const unsigned so2 = (unsigned)(idx >> 3) * 64 * 7168 + (idx & 7) * 896;
                stream<D_B>(mkrsrc(p1, DM * FF * 2), ga, gb, so2, 7168, L_X, 16, cnt, pa);
                STAMP(2);
                wait_ge(FL + F_WQDONE, 2);
                stream<D_B>(mkrsrc(p2, DM * FF * 2), ga, gb, so2, 7168, L_Y, 16, cnt, pa);
                STAMP(3);
                wait_ge(FL + F_GDONE, 14);
                const v4i dr = mkrsrc(p3, FF * DM * 2);
                const unsigned so3 = (unsigned)ks * 56 * 2048 + ct2 * 1024;
                stream<D_D>(dr, va, vb, so3, 2048, L_X, 14, cnt, pa);
                VMWAIT(24);
                asm volatile("ds_max_i32 %0, %1" ::"v"(pa), "v"(cnt - 24) : "memory");
            }
            STAMP(4);
            VMWAIT(0);
            STAMP(5);
            BARRIER();
            STAMP_FLUSH();
            return;
        }
    }
    const int xc = b & 7, bi = b >> 3;
    const bool att = bi & 1;
    const int idx = xc * 16 + (bi >> 1);
    const int ks = idx >> 1, ct2 = idx & 1;
    const int lj = lane & 15, lg = lane >> 4;
    const int q4 = lj >> 2, p4 = lane & 3;
    const unsigned key = (unsigned)(q4 | ((lg & 1) << 2));
    const __amdgpu_buffer_rsrc_t wsr = __builtin_amdgcn_make_buffer_rsrc((void*)ws, 0, (int)WS_END, 0x00020000);

    if (wave == 1) {
        asm volatile("s_waitcnt vmcnt(0)" : "+v"(xa), "+v"(xb), "+v"(ep)::"memory");
        float v[16];
#pragma unroll
        for (int i = 0; i < 4; ++i) { v[2 * i] = bflo(xa[i]); v[2 * i + 1] = bfhi(xa[i]); v[8 + 2 * i] = bflo(xb[i]); v[9 + 2 * i] = bfhi(xb[i]); }
        float s = 0.f;
#pragma unroll
        for (int i = 0; i < 16; ++i) s += v[i];
        const float mean = wave_sum(s) * (1.0f / DM);
        float q = 0.f;
#pragma unroll
        for (int i = 0; i < 16; ++i) { const float d = v[i] - mean; q = fmaf(d, d, q); }
        const float var = wave_sum(q) * (1.0f / DM);
        const float mb = rbf(mean), den = rbf(__builtin_amdgcn_sqrtf(rbf(rbf(var) + 1e-5f)));
        const float rden = __builtin_amdgcn_rcpf(den);
        v4u oa, ob;
#pragma unroll
        for (int i = 0; i < 4; ++i) {
            const unsigned da = pkbf(v[2 * i] - mb, v[2 * i + 1] - mb), db = pkbf(v[8 + 2 * i] - mb, v[9 + 2 * i] - mb);
            oa[i] = pkbf(bflo(da) * rden, bfhi(da) * rden);
            ob[i] = pkbf(bflo(db) * rden, bfhi(db) * rden);
        }
        *(LAS v4u*)(L + L_XN + lane * 16) = oa;
        *(LAS v4u*)(L + L_XN + 1024 + lane * 16) = ob;
        const unsigned tag = ep + 1u, tagw = tag << 16;
        if (lane == 0) FL[F_TAG] = tag;
        SIGNAL(F_XN);
        STAMP(1);
        const int own = xc * 32 + bi, col = own * 4 + ((lg & 1) << 1) + (lg >> 1);
        const float xres = bflo(x[col]);
        u16* outp = out + col;
        asm volatile("" : "+v"(outp));
        if (att) {
            BARRIER();
            BARRIER();
            __builtin_amdgcn_s_sleep(S2_SLEEP);
            wait_until(tstart + S2_T);
            STAMP(2);
            v4u r[8], mlv;
            bool ok[8], okm = false;
#pragma unroll
            for (int i = 0; i < 8; ++i) ok[i] = false;
#pragma nounroll
            for (unsigned sp = 0; sp < GLB_SPIN; ++sp) {
                if (!okm) mlv = LD(((ks >> 4) * 32 + (lane & 31)) * 128 + (((ks >> 2) & 3) * 2 + (lane >> 5)) * 16, WS_S2M);
#pragma unroll
                for (int i = 0; i < 8; ++i) if (!ok[i]) r[i] = LD(ks * 8192 + lane * 16, WS_S2O + i * 1024);
                okm = (mlv.y == tag) & (mlv.w == tag);
                bool all = okm;
#pragma unroll
                for (int i = 0; i < 8; ++i) { ok[i] = tag_ok(r[i], tagw); all &= ok[i]; }
                if (__all(all)) break;
                CBAR();
            }
            STAMP(3);
            float Mx = uf(mlv.x);
            Mx = fmx(Mx, dppm<0xB1>(Mx)); Mx = fmx(Mx, dppm<0x4E>(Mx)); Mx = fmx(Mx, dppm<0x141>(Mx)); Mx = fmx(Mx, dppm<0x128>(Mx));
            { auto t = __builtin_amdgcn_permlane16_swap(fu(Mx), fu(Mx), false, false); Mx = fmx(uf(t[0]), uf(t[1])); }
            float wgt = __builtin_amdgcn_exp2f(uf(mlv.x) - Mx);
            float Ls = row_sum(wgt * uf(mlv.z));
            { auto t = __builtin_amdgcn_permlane16_swap(fu(Ls), fu(Ls), false, false); Ls = uf(t[0]) + uf(t[1]); }
            wgt *= __builtin_amdgcn_rcpf(Ls);
            f32x4 acc = {0.f, 0.f, 0.f, 0.f};
#pragma unroll
            for (int i = 0; i < 8; ++i) {
                const float w = uf(__builtin_amdgcn_ds_bpermute((4 * i + lg + (lj & 4) * 8) * 4, fu(wgt)));
                acc[0] = fmaf(w, g_a(r[i].x), acc[0]); acc[1] = fmaf(w, g_b(r[i].x, r[i].y), acc[1]);
                acc[2] = fmaf(w, g_a(r[i].z), acc[2]); acc[3] = fmaf(w, g_b(r[i].z, r[i].w), acc[3]);
            }
#pragma unroll
            for (int k = 0; k < 4; ++k) acc[k] = xrow_sum(acc[k]);
            if (lane < 16) *(LAS v2u*)(L + L_O + (lj & 4) * 16 + (lj >> 3) * 32 + (lj & 3) * 8) = v2u{pkbf(acc[0], acc[1]), pkbf(acc[2], acc[3])};
        } else {
            wait_ge(FL + F_PROG, 160 + SH_AT);
            if (SH_SLEEP) __builtin_amdgcn_s_sleep(SH_SLEEP);
            wait_until(tstart + SH_T);
            STAMP(2);
            const int un = lane & 31;
            const unsigned off = ((lane >> 5) * FF + ks * 56 + (un < 28 ? un : 27) * 2) * 8;
            v4u r[8];
            bool ok[8];
#pragma unroll
            for (int i = 0; i < 8; ++i) ok[i] = false;
#pragma nounroll
            for (unsigned sp = 0; sp < GLB_SPIN; ++sp) {
#pragma unroll
                for (int i = 0; i < 8; ++i) if (!ok[i]) r[i] = LD(off, WS_SH + i * 2 * FF * 8);
                bool all = true;
#pragma unroll
                for (int i = 0; i < 8; ++i) { ok[i] = tag_ok(r[i], tagw); all &= ok[i]; }
                if (__all(all)) break;
                CBAR();
            }
            STAMP(3);
            float hv[2];
#pragma unroll
            for (int c = 0; c < 2; ++c) {
                float g = 0.f, u = 0.f;
#pragma unroll
                for (int i = 0; i < 8; ++i) { g += g_a(r[i][2 * c]); u += g_b(r[i][2 * c], r[i][2 * c + 1]); }
                g = rbf(half_sum(g)); u = rbf(half_sum(u));
                hv[c] = rbf(g * __builtin_amdgcn_rcpf(1.0f + __builtin_amdgcn_exp2f(-g * LOG2E))) * u;
            }
            if (lane < 32) *(LAS unsigned*)(L + L_O + lane * 4) = lane < 28 ? pkbf(hv[0], hv[1]) : 0u;
        }
        STAMP(4);
        BARRIER();
        __builtin_amdgcn_s_sleep(S3_SLEEP);
        wait_until(tstart + S3_T);
        v4u r0, r1;
        bool ok0 = false, ok1 = false;
#pragma nounroll
        for (unsigned sp = 0; sp < GLB_SPIN; ++sp) {
            if (!ok0) r0 = LD(lane * 4096 + own * 16, WS_S3);
            if (!ok1) r1 = LD(lane * 4096 + own * 16, WS_S3 + 64 * 4096);
            ok0 = tag_ok(r0, tagw); ok1 = tag_ok(r1, tagw);
            if (__all(ok0 & ok1)) break;
            CBAR();
        }
        STAMP(5);
        const float s0 = g_a(r0.x) + g_a(r1.x), s1 = g_b(r0.x, r0.y) + g_b(r1.x, r1.y);
        const float s2 = g_a(r0.z) + g_a(r1.z), s3 = g_b(r0.z, r0.w) + g_b(r1.z, r1.w);
        auto t01 = __builtin_amdgcn_permlane32_swap(fu(s0), fu(s1), false, false);
        auto t23 = __builtin_amdgcn_permlane32_swap(fu(s2), fu(s3), false, false);
        auto t = __builtin_amdgcn_permlane16_swap(fu(uf(t01[0]) + uf(t01[1])), fu(uf(t23[0]) + uf(t23[1])), false, false);
        const float tot = row_sum(uf(t[0]) + uf(t[1])) + xres;
        if (lj == 0) {
            *(GAS __bf16*)outp = (__bf16)tot;
            if (b == 0 && lane == 0) __hip_atomic_store((GAS unsigned*)(ws + WS_EPOCH), tag, __ATOMIC_RELAXED, __HIP_MEMORY_SCOPE_AGENT);
        }
        STAMP(6);
        STAMP_FLUSH();
        STAMP_OUT();
        return;
    }
    if (att && wave < 6) { STAMP_FLUSH(); return; }
    wait_ge(FL + F_XN, 1);
    const unsigned tag = FL[F_TAG], tagw = tag << 16;
    if ((wave & 14) == 6) {
        const int c = wave & 1;
        const unsigned row = 4 * lg + q4;
        const unsigned a0 = L_Y + row * 128 + (((2 * c) ^ ((row >> 1) & 3)) << 5) + p4 * 8, a1 = a0 ^ 32u;
        const unsigned xoff = L_XN + (b >> 6) * 512 + lg * 8;
        unsigned dst = ((b >> 6) * 4096 + (b & 63) * 64 + c * 32 + lj * 16 + lg * 4) * 4 + WS_S1;
        PIN(dst);
        f32x4 acc0 = {0.f, 0.f, 0.f, 0.f}, acc1 = {0.f, 0.f, 0.f, 0.f};
#pragma nounroll
        for (int h = 0; h < 4; ++h) {
            v4u xq[2], af[2], ag[2];
#pragma unroll
            for (int s = 0; s < 2; ++s) xq[s] = xfrag(L, xoff + h * 128 + s * 64);
            wait_ge(FL + F_PROG, 8 * h + 8);
#pragma unroll
            for (int s = 0; s < 2; ++s) { af[s] = trfrag(L, a0 + h * 8192 + s * 4096, 2048); ag[s] = trfrag(L, a1 + h * 8192 + s * 4096, 2048); }
            __builtin_amdgcn_sched_barrier(0);
#pragma unroll
            for (int s = 0; s < 2; ++s) { acc0 = mfma(af[s], xq[s], acc0); acc1 = mfma(ag[s], xq[s], acc1); }
        }
        const f32x4 acc = lj == 0 ? acc0 : acc1;
        if (lj < 2) ST(pack4(acc[0], acc[1], acc[2], acc[3], tagw), dst);
        SIGNAL(F_WQDONE);
        STAMP(1);
        if (att) { STAMP_FLUSH(); return; }
    }
    const unsigned rbase = (4 * lg + q4) * 1024 + p4 * 8;
    if (att) {
        const int aw = wave - 8, gkv = xc >> 1, split = idx & 31;
        unsigned a_q = L_Q + (lj & 7) * 272 + lg * 16, a_k = L_X + (16 * aw + lj) * 256, a_v = L_X + 32768 + (4 * lg + q4) * 256 + ((aw ^ key) << 5) + p4 * 8;
        unsigned a_p = L_P + lane * 8, a_ml = L_ML + (lj & 7) * 80;
        unsigned dst = (((gkv * 4 + (lj >> 1)) * 4 + (aw >> 1)) * 32 + split) * 256 + (aw & 1) * 128 + (lj & 1) * 64 + lg * 16 + WS_S2O;
        unsigned k0 = a_k + ((lg ^ lj) << 4), k1 = a_k + (((4 + lg) ^ lj) << 4), k2 = a_k + (((8 + lg) ^ lj) << 4), k3 = a_k + (((12 + lg) ^ lj) << 4);
        PIN(a_q); PIN(a_v); PIN(a_p); PIN(a_ml); PIN(dst);
        wait_ge(FL + F_PROG, S1_AT);
        STAMP(1);
        v4u kf[4];
        kf[0] = *(LAS v4u*)(L + k0); kf[1] = *(LAS v4u*)(L + k1); kf[2] = *(LAS v4u*)(L + k2); kf[3] = *(LAS v4u*)(L + k3);
        {
            v4u r0, r1;
#pragma nounroll
            for (unsigned sp = 0; sp < GLB_SPIN; ++sp) {
                r0 = LD((lane >> 5) * 16384 + gkv * 4096 + aw * 512 + (lane & 31) * 16, WS_S1);
                r1 = LD((lane >> 5) * 16384 + gkv * 4096 + aw * 512 + (lane & 31) * 16, WS_S1 + 32768);
                if (__all(tag_ok(r0, tagw) & tag_ok(r1, tagw))) break;
                CBAR();
            }
            STAMP(2);
            const float q0 = half_sum(g_a(r0.x) + g_a(r1.x)), q1 = half_sum(g_b(r0.x, r0.y) + g_b(r1.x, r1.y));
            const float q2 = half_sum(g_a(r0.z) + g_a(r1.z)), q3 = half_sum(g_b(r0.z, r0.w) + g_b(r1.z, r1.w));
            if (lane < 32) *(LAS v2u*)(L + L_Q + aw * 272 + lane * 8) = v2u{pkbf(q0, q1), pkbf(q2, q3)};
        }
        BARRIER();
        STAMP(3);
        v4u qf[4];
#pragma unroll
        for (int s = 0; s < 4; ++s) qf[s] = *(LAS v4u*)(L + a_q + s * 64);
        __builtin_amdgcn_sched_barrier(0);
        f32x4 sc = {0.f, 0.f, 0.f, 0.f};
#pragma unroll
        for (int s = 0; s < 4; ++s) sc = mfma(kf[s], qf[s], sc);
        __builtin_amdgcn_sched_barrier(0);
        v2u vf[8];
#pragma unroll
        for (int t = 0; t < 8; ++t) vf[t] = trread(L, a_v + t * 4096);
        __builtin_amdgcn_sched_barrier(0);
        {
            const unsigned r01 = pkbf(sc[0], sc[1]), r23 = pkbf(sc[2], sc[3]);
            const float SC = 0.08838834764831845f;
            const unsigned s01 = pkbf(bflo(r01) * SC, bfhi(r01) * SC), s23 = pkbf(bflo(r23) * SC, bfhi(r23) * SC);
            sc[0] = bflo(s01); sc[1] = bfhi(s01); sc[2] = bflo(s23); sc[3] = bfhi(s23);
        }
        const float m = xrow_max(fmx(fmx(sc[0], sc[1]), fmx(sc[2], sc[3]))) * LOG2E;
#pragma unroll
        for (int r = 0; r < 4; ++r) sc[r] = __builtin_amdgcn_exp2f(fmaf(sc[r], LOG2E, -m));
        *(LAS v2u*)(L + a_p + aw * 512) = v2u{pkbf(sc[0], sc[1]), pkbf(sc[2], sc[3])};
        const float l = xrow_sum((sc[0] + sc[1]) + (sc[2] + sc[3]));
        if (lane < 8) *(LAS v2u*)(L + a_ml + aw * 8) = v2u{fu(m), fu(l)};
        BARRIER();
        STAMP(4);
        v2u pf[8];
        f32x4 ml[4];
#pragma unroll
        for (int t = 0; t < 4; ++t) ml[t] = *(LAS f32x4*)(L + a_ml + t * 16);
#pragma unroll
        for (int t = 0; t < 8; ++t) pf[t] = *(LAS v2u*)(L + a_p + t * 512);
        __builtin_amdgcn_sched_barrier(0);
        f32x4 ot[8];
#pragma unroll
        for (int t = 0; t < 8; ++t) ot[t] = mfma16(vf[t], pf[t], f32x4{0.f, 0.f, 0.f, 0.f});
        const float M = fmx(fmx(fmx(ml[0][0], ml[0][2]), fmx(ml[1][0], ml[1][2])), fmx(fmx(ml[2][0], ml[2][2]), fmx(ml[3][0], ml[3][2])));
        f32x4 o = {0.f, 0.f, 0.f, 0.f};
        float lt = 0.f;
#pragma unroll
        for (int t = 0; t < 8; ++t) {
            const float w = __builtin_amdgcn_exp2f(ml[t >> 1][(t & 1) * 2] - M);
            lt = fmaf(w, ml[t >> 1][(t & 1) * 2 + 1], lt);
            o += w * ot[t];
        }
        if (lj < 8) ST(pack4(o[0], o[1], o[2], o[3], tagw), dst);
        if (aw == 0 && lane < 8) ST((v4u{fu(M), tag, fu(lt), tag}), (gkv * 32 + split) * 128 + lj * 16 + WS_S2M);
        STAMP(5);
    } else {
        const int gw = wave - 2;
        unsigned a0 = rbase + (((2 * gw) ^ key) << 5), a1 = rbase + (((2 * gw + 1) ^ key) << 5);
        unsigned dst = ((idx >> 3) * FF + (idx & 7) * 448 + gw * 32 + (lj >> 1) * 16 + lg * 4 + (lj & 1) * 2) * 8 + WS_SH;
        PIN(dst);
        v4u xf[2], af[2][2];
#pragma unroll
        for (int s = 0; s < 2; ++s) xf[s] = xfrag(L, L_XN + (idx >> 3) * 128 + lg * 8 + s * 64);
        wait_ge(FL + F_PROG, 96);
#pragma unroll
        for (int s = 0; s < 2; ++s) { af[s][0] = trfrag(L, L_X + a0 + s * 32768, 16384); af[s][1] = trfrag(L, L_X + a1 + s * 32768, 16384); }
        SIGNAL(F_GDONE);
        f32x4 g0 = mfma(af[0][0], xf[0], f32x4{0.f, 0.f, 0.f, 0.f}), g1 = mfma(af[0][1], xf[0], f32x4{0.f, 0.f, 0.f, 0.f});
        g0 = mfma(af[1][0], xf[1], g0); g1 = mfma(af[1][1], xf[1], g1);
        wait_ge(FL + F_PROG, 128);
        f32x4 u0 = mfma(trfrag(L, L_Y + a0, 16384), xf[0], f32x4{0.f, 0.f, 0.f, 0.f}), u1 = mfma(trfrag(L, L_Y + a1, 16384), xf[0], f32x4{0.f, 0.f, 0.f, 0.f});
        const bool hi = lj & 1;
        const f32x4 ga = lj < 2 ? g0 : g1;
        float ge = hi ? ga[2] : ga[0], go = hi ? ga[3] : ga[1];
        PIN(ge); PIN(go);
        wait_ge(FL + F_PROG, 160);
        STAMP(2);
        u0 = mfma(trfrag(L, L_Y + 32768 + a0, 16384), xf[1], u0); u1 = mfma(trfrag(L, L_Y + 32768 + a1, 16384), xf[1], u1);
        const f32x4 ua = lj < 2 ? u0 : u1;
        if (lj < 4) ST(pack4(ge, hi ? ua[2] : ua[0], go, hi ? ua[3] : ua[1], tagw), dst);
        STAMP(3);
        if (wave < 8) { STAMP_FLUSH(); return; }
    }
    {
        const int fw = wave - 8;
        const unsigned tb = (att ? L_Y : L_X) + rbase;
        unsigned a[4], dst = (((att ? 0 : 64) + ks) * 1024 + ct2 * 512 + fw * 64 + lj * 16 + lg * 4) * 4 + WS_S3;
        unsigned keep = (att || lg < 2) ? 0xffffffffu : 0u;
#pragma unroll
        for (int t = 0; t < 4; ++t) a[t] = tb + (((4 * fw + t) ^ key) << 5);
        PIN(dst); PIN(keep);
        v4u af[4], ag[4];
        wait_ge(FL + F_PROG, (att ? 96 : 160) + 32);
#pragma unroll
        for (int t = 0; t < 4; ++t) af[t] = trfrag(L, a[t], 16384);
        BARRIER();
        STAMP(6);
        const v4u h0 = xfrag(L, L_O + lg * 8), h1 = xfrag(L, L_O + lg * 8 + 64);
#pragma unroll
        for (int t = 0; t < 4; ++t) ag[t] = trfrag(L, a[t] + 32768, 16384);
        __builtin_amdgcn_sched_barrier(0);
        f32x4 c[4];
#pragma unroll
        for (int t = 0; t < 4; ++t) c[t] = mfma(af[t], h0, f32x4{0.f, 0.f, 0.f, 0.f});
#pragma unroll
        for (int t = 0; t < 4; ++t) { ag[t].z &= keep; ag[t].w &= keep; c[t] = mfma(ag[t], h1, c[t]); }
        const f32x4 cs = lj == 0 ? c[0] : lj == 1 ? c[1] : lj == 2 ? c[2] : c[3];
        if (lj < 4) ST(pack4(cs[0], cs[1], cs[2], cs[3], tagw), dst);
        STAMP(7);
    }
    STAMP_FLUSH();
}

extern "C" void kernel_launch(void* const* d_in, const int* in_sizes, int n_in, void* d_out, int out_size, void* d_ws, size_t ws_size,
                              hipStream_t stream) {
    (void)in_sizes; (void)n_in; (void)out_size; (void)ws_size;
    (void)hipFuncSetAttribute((const void*)fused_decode, hipFuncAttributeMaxDynamicSharedMemorySize, (int)LDS_BYTES);
    fused_decode<<<256, 1024, LDS_BYTES, stream>>>((const u16*)d_in[3], (const u16*)d_in[0], (unsigned char*)d_ws + WS_OFF, (const u16*)d_in[1],
                                                   (const u16*)d_in[2], (const u16*)d_in[4], (const u16*)d_in[6], (const u16*)d_in[7],
                                                   (const u16*)d_in[8], (u16*)d_out);
}
```

```cpp
#define PW 0
#define PI 0
#define PK 3
#define STAMPS
#include <hip/hip_runtime.h>
#include <hip/hip_bf16.h>
#include <stdint.h>

#define LAS __attribute__((address_space(3)))
#define GAS __attribute__((address_space(1)))
typedef unsigned short u16;
typedef unsigned v4u __attribute__((ext_vector_type(4)));
typedef unsigned v2u __attribute__((ext_vector_type(2)));
typedef int v4i __attribute__((ext_vector_type(4)));
typedef float f32x4 __attribute__((ext_vector_type(4)));
typedef float f32x2 __attribute__((ext_vector_type(2)));
typedef short s16x4 __attribute__((ext_vector_type(4)));
typedef __bf16 bf16x8 __attribute__((ext_vector_type(8)));
typedef __bf16 bf16x2 __attribute__((ext_vector_type(2)));

constexpr int DM = 1024, SK = 32768, WIN = 4096, FF = 3584;
constexpr unsigned WS_EPOCH = 0;
constexpr unsigned WS_S1 = 4096;
constexpr unsigned WS_SH = WS_S1 + 4 * 4096 * 4;
constexpr unsigned WS_S2O = WS_SH + 16 * 3584 * 8;
constexpr unsigned WS_S2M = WS_S2O + 64 * 32 * 256;
constexpr unsigned WS_S3 = WS_S2M + 32 * 32 * 16;
constexpr unsigned WS_END = WS_S3 + 128 * 1024 * 4;
constexpr unsigned WS_STAMP = 8u << 20;
constexpr unsigned L_X = 0;
constexpr unsigned L_Y = 65536;
constexpr unsigned L_XN = 131072;
constexpr unsigned L_Q = L_XN + 2048;
constexpr unsigned L_P = L_Q + 2304;
constexpr unsigned L_ML = L_P + 4096;
constexpr unsigned L_O = L_ML + 1024;
constexpr unsigned L_FLAG = L_O + 128;
constexpr unsigned L_JUNK = L_FLAG + 64;
constexpr unsigned L_TS = L_JUNK + 1024;
constexpr unsigned LDS_BYTES = L_TS + 1024;
enum { F_PROG = 0, F_G = 1, F_TAG = 2, F_WQDONE = 3, F_GDONE = 4, F_XN = 5 };

#define PARAM(name, value) constexpr int name = value
PARAM(D_Q, 28);  PARAM(D_A, 24);  PARAM(D_W, 20);  PARAM(D_B, 32);  PARAM(D_D, 24);
PARAM(SH_SLEEP, 8);  PARAM(S2_SLEEP, 68);  PARAM(S3_SLEEP, 26);
constexpr unsigned LDS_SPIN = 1u << 18, GLB_SPIN = 1u << 16;
constexpr int AUX_SC1 = 16;
constexpr float LOG2E = 1.4426950408889634f;

__device__ __forceinline__ float bflo(unsigned u) { return __builtin_bit_cast(float, u << 16); }
__device__ __forceinline__ float bfhi(unsigned u) { return __builtin_bit_cast(float, u & 0xffff0000u); }
__device__ __forceinline__ float rbf(float f) { return (float)(__bf16)f; }
__device__ __forceinline__ unsigned pkbf(float lo, float hi) { bf16x2 v = {(__bf16)lo, (__bf16)hi}; return __builtin_bit_cast(unsigned, v); }
__device__ __forceinline__ unsigned fu(float f) { return __builtin_bit_cast(unsigned, f); }
__device__ __forceinline__ float uf(unsigned u) { return __builtin_bit_cast(float, u); }
__device__ __forceinline__ float fmx(float a, float b) { return __builtin_elementwise_maximum(a, b); }
__device__ __forceinline__ float expdiff(float a, float mx) { return __builtin_amdgcn_exp2f((a - mx) * LOG2E); }
constexpr unsigned TAGM = 0x3ffu;
__device__ __forceinline__ unsigned tw(float a, unsigned tagw) { return (fu(a) & ~TAGM) | tagw; }
__device__ __forceinline__ v4u pack4(float a, float b, float c, float d, unsigned tagw) { return v4u{tw(a, tagw), tw(b, tagw), tw(c, tagw), tw(d, tagw)}; }
__device__ __forceinline__ float val(unsigned w) { return uf(w); }
__device__ __forceinline__ unsigned low(v4u r) { return (r.x | r.y | r.z) | r.w; }
__device__ __forceinline__ bool fresh(v4u r) { return (low(r) & TAGM) == 0u; }

template <int CTRL> __device__ __forceinline__ float dppm(float v) {
    return __builtin_bit_cast(float, __builtin_amdgcn_mov_dpp(__builtin_bit_cast(int, v), CTRL, 0xf, 0xf, true));
}
__device__ __forceinline__ float row_sum(float v) {
    v += dppm<0xB1>(v); v += dppm<0x4E>(v); v += dppm<0x141>(v); v += dppm<0x128>(v);
    return v;
}
__device__ __forceinline__ float half_sum(float x) {
    auto t = __builtin_amdgcn_permlane32_swap(fu(x), fu(x), false, false);
    return uf(t[0]) + uf(t[1]);
}
__device__ __forceinline__ float xrow_max(float x) {
    auto s = __builtin_amdgcn_permlane16_swap(fu(x), fu(x), false, false);
    x = fmx(uf(s[0]), uf(s[1]));
    auto t = __builtin_amdgcn_permlane32_swap(fu(x), fu(x), false, false);
    return fmx(uf(t[0]), uf(t[1]));
}
__device__ __forceinline__ float xrow_sum(float x) {
    auto s = __builtin_amdgcn_permlane16_swap(fu(x), fu(x), false, false);
    return half_sum(uf(s[0]) + uf(s[1]));
}
__device__ __forceinline__ float wave_sum(float v) { return xrow_sum(row_sum(v)); }
__device__ __forceinline__ float wave_max(float x) {
    x = fmx(x, dppm<0xB1>(x)); x = fmx(x, dppm<0x4E>(x)); x = fmx(x, dppm<0x141>(x)); x = fmx(x, dppm<0x128>(x));
    return xrow_max(x);
}
__device__ __forceinline__ f32x4 mfma(v4u a, v4u b, f32x4 c) {
    return __builtin_amdgcn_mfma_f32_16x16x32_bf16(__builtin_bit_cast(bf16x8, a), __builtin_bit_cast(bf16x8, b), c, 0, 0, 0);
}
__device__ __forceinline__ f32x4 mfma16(v2u a, v2u b, f32x4 c) {
    return __builtin_amdgcn_mfma_f32_16x16x16bf16_1k(__builtin_bit_cast(s16x4, a), __builtin_bit_cast(s16x4, b), c, 0, 0, 0);
}
__device__ __forceinline__ v2u trread(LAS unsigned char* L, unsigned off) {
    return __builtin_bit_cast(v2u, __builtin_amdgcn_ds_read_tr16_b64_v4i16((LAS s16x4*)(L + off)));
}
__device__ __forceinline__ v4u trfrag(LAS unsigned char* L, unsigned off, unsigned toff) {
    const v2u a = trread(L, off), b = trread(L, off + toff);
    return v4u{a.x, a.y, b.x, b.y};
}
__device__ __forceinline__ v4u xfrag(LAS unsigned char* L, unsigned off) {
    const v2u a = *(LAS v2u*)(L + off), b = *(LAS v2u*)(L + off + 32);
    return v4u{a.x, a.y, b.x, b.y};
}
#define CBAR() asm volatile("" ::: "memory")
#define LGKM0() asm volatile("s_waitcnt lgkmcnt(0)" ::: "memory")
#define BARRIER() asm volatile("s_waitcnt lgkmcnt(0)\n\ts_barrier" ::: "memory")
#define PIN(v) asm volatile("" : "+v"(v))
__device__ __forceinline__ void wait_ge_at(unsigned addr, unsigned want) {
    unsigned v, sv, n = LDS_SPIN;
    asm volatile("s_branch 1f\n"
                 "0:\n\t"
                 "s_sleep %5\n"
                 "1:\n\t"
                 "ds_read_b32 %0, %3\n\t"
                 "s_sub_u32 %2, %2, 1\n\t"
                 "s_cbranch_scc1 2f\n\t"
                 "s_waitcnt lgkmcnt(0)\n\t"
                 "v_readfirstlane_b32 %1, %0\n\t"
                 "s_cmp_lt_u32 %1, %4\n\t"
                 "s_cbranch_scc1 0b\n"
                 "2:\n\t"
                 "s_waitcnt lgkmcnt(0)"
                 : "=&v"(v), "=&s"(sv), "+&s"(n) : "v"(addr), "s"(want), "i"(1) : "memory", "scc");
}
__device__ __forceinline__ void spin_ge_at(unsigned addr, unsigned want) {
    unsigned v, sv, n = LDS_SPIN;
    asm volatile("1:\n\t"
                 "ds_read_b32 %0, %3\n\t"
                 "s_sub_u32 %2, %2, 1\n\t"
                 "s_cbranch_scc1 2f\n\t"
                 "s_waitcnt lgkmcnt(0)\n\t"
                 "v_readfirstlane_b32 %1, %0\n\t"
                 "s_cmp_lt_u32 %1, %4\n\t"
                 "s_cbranch_scc1 1b\n"
                 "2:\n\t"
                 "s_waitcnt lgkmcnt(0)"
                 : "=&v"(v), "=&s"(sv), "+&s"(n) : "v"(addr), "s"(want) : "memory", "scc");
}
#define wait_ge(ptr, want) wait_ge_at(L_FLAG + 4u * (unsigned)((ptr) - FL), (want))
#define SIGNAL(flag) asm volatile("s_waitcnt lgkmcnt(0)\n\tds_add_u32 %0, %1 offset:%2" ::"v"(lane == 0 ? L_FLAG : L_JUNK + 4u * lane), "v"(1u), "i"(4 * (flag)) : "memory")
__device__ __forceinline__ v4i mkrsrc(const void* p, unsigned bytes) {
    const uint64_t a = (uint64_t)p;
    return v4i{(int)(unsigned)a, (int)((unsigned)(a >> 32) & 0xffffu), (int)bytes, 0x00020000};
}
#define VMWAIT(n) asm volatile("s_waitcnt vmcnt(%0)" ::"i"(n) : "memory")
#define LDRAW(voff, soff) __builtin_amdgcn_raw_buffer_load_b128(wsr, (voff), (soff), AUX_SC1)
#define ST(v, voff) __builtin_amdgcn_raw_buffer_store_b128((v), wsr, (voff), 0, AUX_SC1)

#define STAMP(i) do { ts[i] = __builtin_amdgcn_s_memrealtime(); } while (0)
#define STAMP_FLUSH() do { if (lane == 0) { _Pragma("unroll") for (int i_ = 0; i_ < 8; ++i_) ((LAS unsigned long long*)(L + L_TS))[wave * 8 + i_] = ts[i_]; } } while (0)
#define STAMP_OUT() do { LGKM0(); __builtin_amdgcn_s_sleep(20); ((GAS v4u*)(ws + WS_STAMP))[b * 64 + lane] = ((LAS v4u*)(L + L_TS))[lane]; } while (0)

template <int N, unsigned BASE, unsigned STEP>
__device__ __forceinline__ void gather(__amdgpu_buffer_rsrc_t wsr, unsigned off, unsigned tagw, v4u (&r)[N]) {
#pragma unroll
    for (int i = 0; i < N; ++i) r[i] = LDRAW(off, BASE + i * STEP);
    unsigned bad = 0;
#pragma unroll
    for (int i = 0; i < N; ++i) { r[i] ^= tagw; bad |= low(r[i]); }
    if (__builtin_expect(__all((bad & TAGM) == 0u), 1)) return;
    unsigned pend = 0;
#pragma unroll
    for (int i = 0; i < N; ++i) pend |= __all(fresh(r[i])) ? 0u : 1u << i;
#pragma nounroll
    for (unsigned sp = 0; sp < GLB_SPIN && pend; ++sp) {
        CBAR();
#pragma unroll
        for (int i = 0; i < N; ++i) if (pend & (1u << i)) r[i] = LDRAW(off, BASE + i * STEP);
        VMWAIT(0);
#pragma unroll
        for (int i = 0; i < N; ++i) if (pend & (1u << i)) { r[i] ^= tagw; if (__all(fresh(r[i]))) pend &= ~(1u << i); }
    }
}

template <int DEP>
__device__ __forceinline__ void dma4(v4i rs, v4u v, unsigned& soff, unsigned sstep, unsigned lds) {
    asm volatile("s_mov_b32 m0, %[lds]\n\t"
                 "s_nop 0\n\t"
                 "buffer_load_dwordx4 %[v0], %[rs], %[so] offen nt lds\n\t"
                 "s_add_u32 m0, m0, 0x400\n\t"
                 "s_add_u32 %[so], %[so], %[st]\n\t"
                 "buffer_load_dwordx4 %[v1], %[rs], %[so] offen nt lds\n\t"
                 "s_add_u32 m0, m0, 0x400\n\t"
                 "s_add_u32 %[so], %[so], %[st]\n\t"
                 "s_waitcnt vmcnt(%[dep])\n\t"
                 "buffer_load_dwordx4 %[v2], %[rs], %[so] offen nt lds\n\t"
                 "s_add_u32 m0, m0, 0x400\n\t"
                 "s_add_u32 %[so], %[so], %[st]\n\t"
                 "buffer_load_dwordx4 %[v3], %[rs], %[so] offen nt lds\n\t"
                 "s_add_u32 %[so], %[so], %[st]\n\t"
                 "s_waitcnt vmcnt(%[dep])"
                 : [so] "+&s"(soff)
                 : [lds] "s"(lds), [v0] "v"(v.x), [v1] "v"(v.y), [v2] "v"(v.z), [v3] "v"(v.w), [rs] "s"(rs), [st] "s"(sstep), [dep] "i"(DEP)
                 : "memory", "scc");
}
template <int DEP>
__device__ __forceinline__ void stream(v4i rs, v4u va, v4u vb, unsigned soff, unsigned sstep, unsigned lds, int ng, int& prog, unsigned progaddr) {
    const unsigned end = lds + ng * 4096;
#pragma nounroll
    do {
        dma4<DEP>(rs, va, soff, sstep, lds);
        prog += 4;
        asm volatile("ds_max_i32 %0, %1" ::"v"(progaddr), "v"(prog - DEP) : "memory");
        dma4<DEP>(rs, vb, soff, sstep, lds + 4096);
        prog += 4; lds += 8192;
        asm volatile("ds_max_i32 %0, %1" ::"v"(progaddr), "v"(prog - DEP) : "memory");
    } while (lds != end);
}

__global__ void __launch_bounds__(1024, 4)
fused_decode(const u16* __restrict__ wq, const u16* __restrict__ x, unsigned char* ws, const u16* __restrict__ kc, const u16* __restrict__ vc,
             const u16* __restrict__ wo, const u16* __restrict__ wg, const u16* __restrict__ wu, const u16* __restrict__ wd, u16* out) {
    extern __shared__ __attribute__((aligned(16))) unsigned char smem[];
    LAS unsigned char* L = (LAS unsigned char*)smem;
    volatile LAS unsigned* FL = (volatile LAS unsigned*)(L + L_FLAG);
    const int tid = threadIdx.x, lane = tid & 63, wave = __builtin_amdgcn_readfirstlane(tid >> 6), b = blockIdx.x;
    unsigned long long ts[8] = {0, 0, 0, 0, 0, 0, 0, 0};
    STAMP(0);
    FL[lane & 15] = 0u;
    v4u xa, xb;
    v2u xs, xg;
    unsigned ep;
    {
        const int rl = lane >> 3, p = lane & 7;
        const unsigned v0 = rl * 8192 + (((p >> 1) ^ ((rl >> 1) & 3)) << 5) + (p & 1) * 16;
        unsigned so = (unsigned)(b >> 6) * 256 * 8192 + (b & 63) * 128;
        const v4i rs = mkrsrc(wq, DM * 4096 * 2);
        const unsigned l16 = lane * 16, zero = 0;
        const unsigned os = (b >> 6) * 512 + lane * 8, og = ((b & 7) * 2 + (b >> 7)) * 128 + (lane & 15) * 8;
        uint64_t pc;
        unsigned coff = (b >> 3) * 1024 + l16;
        asm volatile("s_cmp_lg_u32 %[wave], 0\n\t"
                     "s_cbranch_scc1 1f\n\t"
                     "s_mov_b32 m0, %[lds]\n"
                     "0:\n\t"
                     "s_nop 0\n\t"
                     "buffer_load_dwordx4 %[v0], %[rs], %[so] offen nt lds\n\t"
                     "s_add_u32 m0, m0, 0x400\n\t"
                     "s_add_u32 %[so], %[so], 0x10000\n\t"
                     "buffer_load_dwordx4 %[v0], %[rs], %[so] offen nt lds\n\t"
                     "s_add_u32 m0, m0, 0x400\n\t"
                     "s_add_u32 %[so], %[so], 0x10000\n\t"
                     "buffer_load_dwordx4 %[v0], %[rs], %[so] offen nt lds\n\t"
                     "s_add_u32 m0, m0, 0x400\n\t"
                     "s_add_u32 %[so], %[so], 0x10000\n\t"
                     "buffer_load_dwordx4 %[v0], %[rs], %[so] offen nt lds\n\t"
                     "s_add_u32 m0, m0, 0x400\n\t"
                     "s_add_u32 %[so], %[so], 0x10000\n\t"
                     "s_cmp_lg_u32 m0, %[lend]\n\t"
                     "s_cbranch_scc1 0b\n\t"
                     "s_branch 3f\n"
                     "1:\n\t"
                     "s_cmp_lg_u32 %[wave], 1\n\t"
                     "s_cbranch_scc1 2f\n\t"
                     "global_load_dwordx4 %[xa], %[l16], %[x]\n\t"
                     "global_load_dwordx4 %[xb], %[l16], %[x] offset:1024\n\t"
                     "global_load_dwordx2 %[xs], %[os], %[x]\n\t"
                     "global_load_dwordx2 %[xg], %[og], %[x]\n\t"
                     "global_load_dword %[ep], %[zero], %[ws] sc1\n\t"
                     "s_branch 3f\n"
                     "2:\n\t"
                     "s_cmp_lg_u32 %[wave], 2\n\t"
                     "s_cbranch_scc1 3f\n\t"
                     "s_mov_b32 m0, %[junk]\n\t"
                     ".subsection 1\n"
                     "5:\n\t"
                     ".subsection 0\n\t"
                     "v_min_u32 %[coff], 5b-4f-16, %[coff]\n\t"
                     "s_getpc_b64 %[pc]\n"
                     "4:\n\t"
                     "global_load_lds_dwordx4 %[coff], %[pc]\n"
                     "3:\n\t"
                     "s_waitcnt lgkmcnt(0)\n\t"
                     "s_barrier"
                     : [so] "+&s"(so), [xa] "=&v"(xa), [xb] "=&v"(xb), [xs] "=&v"(xs), [xg] "=&v"(xg), [ep] "=&v"(ep), [pc] "=&s"(pc), [coff] "+&v"(coff)
                     : [wave] "s"(wave), [lds] "i"(L_Y), [lend] "i"(L_Y + 24576), [junk] "i"(L_JUNK), [v0] "v"(v0), [rs] "s"(rs), [l16] "v"(l16), [zero] "v"(zero), [os] "v"(os), [og] "v"(og), [x] "s"(x), [ws] "s"(ws)
                       , "s"(kc), "s"(vc), "s"(wo), "s"(wg), "s"(wu), "s"(wd), "s"(out)
                     : "memory", "scc");
        const u16 *p1 = (b & 8) ? kc : wg, *p2 = (b & 8) ? vc : wu, *p3 = (b & 8) ? wo : wd;
        if (wave == 0) {
            const int xc = b & 7, bi = b >> 3, idx = xc * 16 + (bi >> 1), ks = idx >> 1, ct2 = idx & 1;
            const int lj = lane & 15, lg = lane >> 4;
            const unsigned pa = lane == 0 ? L_FLAG + 4 * F_PROG : L_JUNK + 4 * lane;
            int cnt = 24;
            PIN(cnt);
            stream<D_Q>(rs, v4u{v0, v0, v0, v0}, v4u{v0, v0, v0, v0}, so, 65536, L_Y + 24576, 2, cnt, pa);
            STAMP(1);
            v4u va, vb;
#pragma unroll
            for (int i = 0; i < 4; ++i) { va[i] = l16 ^ (i << 5); vb[i] = l16 ^ ((i + 4) << 5); }
            if (bi & 1) {
                const size_t kvoff = ((size_t)(xc >> 1) * SK + (SK - WIN) + (idx & 31) * 128) * 128;
                const unsigned k0 = lg * 256 + ((lj ^ lg) << 4);
                const v4u kv = v4u{k0, k0 ^ 0x40u, k0 ^ 0x80u, k0 ^ 0xC0u};
                stream<D_A>(mkrsrc(p1 + kvoff, 32768), kv, kv, 0, 1024, L_X, 8, cnt, pa);
                const unsigned u0 = lg * 256 + (((lj >> 1) ^ lg) << 5) + (lj & 1) * 16;
                const v4u vv = v4u{u0, u0 ^ 0x80u, u0, u0 ^ 0x80u};
                stream<D_A>(mkrsrc(p2 + kvoff, 32768), vv, vv, 0, 1024, L_X + 32768, 8, cnt, pa);
                STAMP(2);
                const v4i wr = mkrsrc(p3, 4096 * DM * 2);
                const unsigned so2 = (unsigned)((ks >> 2) * 256 + (ks & 3) * 32) * 2048 + ct2 * 1024;
                VMWAIT(0);
                STAMP(3);
                BARRIER();
                stream<D_W>(wr, va, vb, so2, 2048, L_Y, 8, cnt, pa);
                stream<D_W>(wr, va, vb, so2 + 128 * 2048, 2048, L_Y + 32768, 8, cnt, pa);
            } else {
                v4u ga, gb;
#pragma unroll
                for (int i = 0; i < 4; ++i) { ga[i] = va[i] < 896 ? va[i] : 0x40000000u; gb[i] = vb[i] < 896 ? vb[i] : 0x40000000u; }
                const unsigned so2 = (unsigned)(idx >> 3) * 64 * 7168 + (idx & 7) * 896;
                stream<D_B>(mkrsrc(p1, DM * FF * 2), ga, gb, so2, 7168, L_X, 16, cnt, pa);
                STAMP(2);
                wait_ge(FL + F_WQDONE, 2);
                stream<D_B>(mkrsrc(p2, DM * FF * 2), ga, gb, so2, 7168, L_Y, 16, cnt, pa);
                STAMP(3);
                wait_ge(FL + F_GDONE, 14);
                const v4i dr = mkrsrc(p3, FF * DM * 2);
                const unsigned so3 = (unsigned)ks * 56 * 2048 + ct2 * 1024;
                stream<D_D>(dr, va, vb, so3, 2048, L_X, 14, cnt, pa);
                VMWAIT(24);
                asm volatile("ds_max_i32 %0, %1" ::"v"(pa), "v"(cnt - 24) : "memory");
            }
            STAMP(4);
            VMWAIT(0);
            STAMP(5);
            asm volatile("s_barrier" ::: "memory");
            STAMP_FLUSH();
            return;
        }
    }
    const int xc = b & 7, bi = b >> 3;
    const bool att = bi & 1;
    const int idx = xc * 16 + (bi >> 1);
    const int ks = idx >> 1, ct2 = idx & 1;
    const int lj = lane & 15, lg = lane >> 4;
    const int q4 = lj >> 2, p4 = lane & 3;
    const unsigned key = (unsigned)(q4 | ((lg & 1) << 2));
    const __amdgpu_buffer_rsrc_t wsr = __builtin_amdgcn_make_buffer_rsrc((void*)ws, 0, (int)WS_END, 0x00020000);

    if (wave == 1) {
        const unsigned long long pbase = __builtin_amdgcn_s_memrealtime();
        asm volatile("s_waitcnt vmcnt(0)" : "+v"(xa), "+v"(xb), "+v"(xs), "+v"(xg), "+v"(ep)::"memory");
        const float c0 = bflo(__builtin_amdgcn_readfirstlane(xa.x));
        f32x2 s1 = {0.f, 0.f}, s2 = {0.f, 0.f};
#pragma unroll
        for (int i = 0; i < 4; ++i) {
            const f32x2 da = f32x2{bflo(xa[i]), bfhi(xa[i])} - c0, db = f32x2{bflo(xb[i]), bfhi(xb[i])} - c0;
            s1 += da + db;
            s2 += da * da; s2 += db * db;
        }
        const float md = wave_sum(s1[0] + s1[1]) * (1.0f / DM), e2 = wave_sum(s2[0] + s2[1]) * (1.0f / DM);
        const float var = fmx(fmaf(-md, md, e2), 0.f);
        const float mb = rbf(c0 + md), den = rbf(__builtin_amdgcn_sqrtf(rbf(rbf(var) + 1e-5f)));
        const float rden = __builtin_amdgcn_rcpf(den);
        v2u oq, og2;
#pragma unroll
        for (int i = 0; i < 2; ++i) {
            unsigned dq = pkbf(bflo(xs[i]) - mb, bfhi(xs[i]) - mb), dg = pkbf(bflo(xg[i]) - mb, bfhi(xg[i]) - mb);
            PIN(dq); PIN(dg);
            oq[i] = pkbf(bflo(dq) * rden, bfhi(dq) * rden);
            og2[i] = pkbf(bflo(dg) * rden, bfhi(dg) * rden);
        }
        *(LAS v2u*)(L + L_XN + lane * 8) = oq;
        *(LAS v2u*)(L + L_XN + 1024 + lane * 8) = og2;
        const unsigned tag = ep + 1u, tagw = tag & TAGM;
        FL[F_TAG] = tag;
        SIGNAL(F_XN);
        STAMP(1);
        const int own = xc * 32 + bi, col = own * 4 + ((lg & 1) << 1) + (lg >> 1);
        unsigned o_s3 = lane * 4096 + own * 16;
        const float xres = bflo(x[col]);
        PIN(o_s3);
        u16* outp = out + col;
        asm volatile("" : "+v"(outp));
        if (att) {
            unsigned a_o = L_O + (lj & 4) * 16 + (lj >> 3) * 32 + (lj & 3) * 8 + ((lg & 1) << 2) + (lg & 2);
            PIN(a_o);
            BARRIER();
            __builtin_amdgcn_s_sleep(S2_SLEEP);
            STAMP(2);
            const unsigned o_ml = ((ks >> 4) * 32 + (lane & 31)) * 128 + (((ks >> 2) & 3) * 2 + (lane >> 5)) * 16, o_r = ks * 8192 + lane * 16;
            v4u r[8], mlv = LDRAW(o_ml, WS_S2M);
            gather<8, WS_S2O, 1024>(wsr, o_r, tagw, r);
#pragma nounroll
            for (unsigned sp = 0; sp < GLB_SPIN; ++sp) {
                if (__builtin_expect(__all(((mlv.y ^ tag) | (mlv.w ^ tag)) == 0u), 1)) break;
                CBAR();
                mlv = LDRAW(o_ml, WS_S2M);
            }
            STAMP(3);
            float Mx = uf(mlv.x);
            Mx = fmx(Mx, dppm<0xB1>(Mx)); Mx = fmx(Mx, dppm<0x4E>(Mx)); Mx = fmx(Mx, dppm<0x141>(Mx)); Mx = fmx(Mx, dppm<0x128>(Mx));
            { auto t = __builtin_amdgcn_permlane16_swap(fu(Mx), fu(Mx), false, false); Mx = fmx(uf(t[0]), uf(t[1])); }
            const float wgt = expdiff(uf(mlv.x), Mx);
            float w[8];
#pragma unroll
            for (int i = 0; i < 8; ++i) w[i] = uf(__builtin_amdgcn_ds_bpermute((lg + (lj & 4) * 8) * 4 + 16 * i, fu(wgt)));
            float Ls = row_sum(wgt * uf(mlv.z));
            { auto t = __builtin_amdgcn_permlane16_swap(fu(Ls), fu(Ls), false, false); Ls = uf(t[0]) + uf(t[1]); }
            Ls = __builtin_amdgcn_rcpf(Ls);
            const float rl = (lj & 4) ? uf(__builtin_amdgcn_readlane(fu(Ls), 32)) : uf(__builtin_amdgcn_readlane(fu(Ls), 0));
            f32x2 a01 = {0.f, 0.f}, a23 = {0.f, 0.f};
#pragma unroll
            for (int i = 0; i < 8; ++i) {
                a01 += w[i] * f32x2{val(r[i].x), val(r[i].y)};
                a23 += w[i] * f32x2{val(r[i].z), val(r[i].w)};
            }
            auto t01 = __builtin_amdgcn_permlane32_swap(fu(a01[0]), fu(a01[1]), false, false);
            auto t23 = __builtin_amdgcn_permlane32_swap(fu(a23[0]), fu(a23[1]), false, false);
            auto tt = __builtin_amdgcn_permlane16_swap(fu(uf(t01[0]) + uf(t01[1])), fu(uf(t23[0]) + uf(t23[1])), false, false);
            *(LAS __bf16*)(L + a_o) = (__bf16)((uf(tt[0]) + uf(tt[1])) * rl);
        } else {
            wait_ge(FL + F_PROG, 192);
            __builtin_amdgcn_s_sleep(SH_SLEEP);
            STAMP(2);
            const int un = lane & 31;
            const unsigned off = ((lane >> 5) * FF + ks * 56 + (un < 28 ? un : 27) * 2) * 8;
            v4u r[8];
            gather<8, WS_SH, 2 * FF * 8>(wsr, off, tagw, r);
            f32x2 c0 = {0.f, 0.f}, c1 = {0.f, 0.f};
#pragma unroll
            for (int i = 0; i < 8; i += 2) {
                c0 += f32x2{val(r[i].x), val(r[i].y)} + f32x2{val(r[i + 1].x), val(r[i + 1].y)};
                c1 += f32x2{val(r[i].z), val(r[i].w)} + f32x2{val(r[i + 1].z), val(r[i + 1].w)};
            }
            STAMP(3);
            auto tg = __builtin_amdgcn_permlane32_swap(fu(c0[0]), fu(c1[0]), false, false);
            auto tu = __builtin_amdgcn_permlane32_swap(fu(c0[1]), fu(c1[1]), false, false);
            const unsigned gu = pkbf(uf(tg[0]) + uf(tg[1]), uf(tu[0]) + uf(tu[1]));
            const float g = bflo(gu);
            const float hv = rbf(g * __builtin_amdgcn_rcpf(1.0f + __builtin_amdgcn_exp2f(-g * LOG2E))) * bfhi(gu);
            *(LAS __bf16*)(L + L_O + un * 4 + (lane >> 5) * 2) = (__bf16)(un < 28 ? hv : 0.f);
        }
        STAMP(4);
        BARRIER();
        {
        __builtin_amdgcn_s_sleep(S3_SLEEP);
        v4u w0, w1;
        unsigned x0, x1, x2, x3, y0, y1, y2, y3;
        {
            const v4i wr4 = mkrsrc(ws, WS_END);
            unsigned n = GLB_SPIN, t0, t1;
            uint64_t m1, m2;
            asm volatile("s_mov_b64 vcc, -1\n\t"
                         "s_mov_b64 %[m1], -1\n"
                         "0:\n\t"
                         "s_mov_b64 exec, vcc\n\t"
                         "s_cbranch_execz 2f\n\t"
                         "buffer_load_dwordx4 %[w0], %[off], %[rs], %[b0] offen sc1\n"
                         "2:\n\t"
                         "s_mov_b64 exec, %[m1]\n\t"
                         "s_cbranch_execz 3f\n\t"
                         "buffer_load_dwordx4 %[w1], %[off], %[rs], %[b1] offen sc1\n"
                         "3:\n\t"
                         "s_mov_b64 exec, -1\n\t"
                         "s_sub_u32 %[n], %[n], 1\n\t"
                         "s_waitcnt vmcnt(0)\n\t"
                         "s_cbranch_scc1 1f\n\t"
                         "v_xor_b32 %[x0], v100, %[tg]\n\t"
                         "v_xor_b32 %[x1], v101, %[tg]\n\t"
                         "v_xor_b32 %[x2], v102, %[tg]\n\t"
                         "v_xor_b32 %[x3], v103, %[tg]\n\t"
                         "v_xor_b32 %[y0], v104, %[tg]\n\t"
                         "v_xor_b32 %[y1], v105, %[tg]\n\t"
                         "v_xor_b32 %[y2], v106, %[tg]\n\t"
                         "v_xor_b32 %[y3], v107, %[tg]\n\t"
                         "v_or3_b32 %[t0], %[x0], %[x1], %[x2]\n\t"
                         "v_or3_b32 %[t1], %[y0], %[y1], %[y2]\n\t"
                         "v_bitop3_b32 %[t0], %[t0], %[mk], %[x3] bitop3:0xc8\n\t"
                         "v_bitop3_b32 %[t1], %[t1], %[mk], %[y3] bitop3:0xc8\n\t"
                         "v_cmp_ne_u32 vcc, 0, %[t0]\n\t"
                         "v_cmp_ne_u32 %[m1], 0, %[t1]\n\t"
                         "s_or_b64 %[m2], vcc, %[m1]\n\t"
                         "s_cbranch_scc1 0b\n"
                         "1:"
                         : [w0] "=&{v[100:103]}"(w0), [w1] "=&{v[104:107]}"(w1), [x0] "=&v"(x0), [x1] "=&v"(x1), [x2] "=&v"(x2), [x3] "=&v"(x3),
                           [y0] "=&v"(y0), [y1] "=&v"(y1), [y2] "=&v"(y2), [y3] "=&v"(y3), [t0] "=&v"(t0), [t1] "=&v"(t1), [m1] "=&s"(m1), [m2] "=&s"(m2), [n] "+&s"(n)
                         : [off] "v"(o_s3), [rs] "s"(wr4), [b0] "s"(WS_S3), [b1] "s"(WS_S3 + 64 * 4096), [tg] "v"(tagw), [mk] "s"(TAGM)
                         : "memory", "scc", "vcc");
        }
        const v4u r0 = {x0, x1, x2, x3}, r1 = {y0, y1, y2, y3};
        STAMP(5);
        const float s0 = val(r0.x) + val(r1.x), s1 = val(r0.y) + val(r1.y);
        const float s2 = val(r0.z) + val(r1.z), s3 = val(r0.w) + val(r1.w);
        auto t01 = __builtin_amdgcn_permlane32_swap(fu(s0), fu(s1), false, false);
        auto t23 = __builtin_amdgcn_permlane32_swap(fu(s2), fu(s3), false, false);
        auto t = __builtin_amdgcn_permlane16_swap(fu(uf(t01[0]) + uf(t01[1])), fu(uf(t23[0]) + uf(t23[1])), false, false);
        const float tot = row_sum(uf(t[0]) + uf(t[1])) + xres;
        if (lj == 0) {
            *(GAS __bf16*)outp = (__bf16)tot;
            if (idx == 0 && !att && lane == 0) __hip_atomic_store((GAS unsigned*)(ws + WS_EPOCH), tag, __ATOMIC_RELAXED, __HIP_MEMORY_SCOPE_AGENT);
        }
        }
        STAMP(6);
        STAMP_FLUSH();
        {
            asm volatile("s_waitcnt lgkmcnt(0)" ::: "memory");
            __builtin_amdgcn_s_sleep(20);
            unsigned long long t = ((volatile LAS unsigned long long*)(L + L_TS))[PW * 8 + PI];
            const bool use = (PK == 2 || (int)att == PK) && PW >= 0;
            unsigned d = use ? (unsigned)__builtin_amdgcn_readfirstlane((unsigned)(t - pbase)) : 0u;
            if (d > 3000u) d = 0u;
            while (__builtin_amdgcn_s_memrealtime() - pbase < 3000ull + 4ull * d) __builtin_amdgcn_s_sleep(1);
        }
        return;
    }
    if (att && wave < 6) { STAMP_FLUSH(); return; }
    wait_ge(FL + F_XN, 1);
    const unsigned tag = FL[F_TAG], tagw = tag & TAGM;
    if ((wave & 14) == 6) {
        const int c = wave & 1;
        const unsigned row = 4 * lg + q4;
        const unsigned a0 = L_Y + row * 128 + (((2 * c) ^ ((row >> 1) & 3)) << 5) + p4 * 8, a1 = a0 ^ 32u;
        unsigned dst = ((b >> 6) * 4096 + (b & 63) * 64 + c * 32 + lj * 16 + lg * 4) * 4 + WS_S1;
        PIN(dst);
        f32x4 acc0 = {0.f, 0.f, 0.f, 0.f}, acc1 = {0.f, 0.f, 0.f, 0.f};
        v4u xq[8], af[6], ag[6];
#pragma unroll
        for (int s = 0; s < 8; ++s) xq[s] = xfrag(L, L_XN + lg * 8 + s * 64);
        wait_ge(FL + F_PROG, 24);
#pragma unroll
        for (int s = 0; s < 6; ++s) { af[s] = trfrag(L, a0 + s * 4096, 2048); ag[s] = trfrag(L, a1 + s * 4096, 2048); }
        __builtin_amdgcn_sched_barrier(0);
#pragma unroll
        for (int s = 0; s < 6; ++s) { acc0 = mfma(af[s], xq[s], acc0); acc1 = mfma(ag[s], xq[s], acc1); }
        __builtin_amdgcn_sched_barrier(0);
        wait_ge(FL + F_PROG, 32);
#pragma unroll
        for (int s = 0; s < 2; ++s) { af[s] = trfrag(L, a0 + (6 + s) * 4096, 2048); ag[s] = trfrag(L, a1 + (6 + s) * 4096, 2048); }
        __builtin_amdgcn_sched_barrier(0);
#pragma unroll
        for (int s = 0; s < 2; ++s) { acc0 = mfma(af[s], xq[6 + s], acc0); acc1 = mfma(ag[s], xq[6 + s], acc1); }
        const f32x4 acc = lj == 0 ? acc0 : acc1;
        if (lj < 2) ST(pack4(uf(fu(acc[0]) + 0x200u), uf(fu(acc[1]) + 0x200u), uf(fu(acc[2]) + 0x200u), uf(fu(acc[3]) + 0x200u), tagw), dst);
        SIGNAL(F_WQDONE);
        STAMP(1);
        if (att) { STAMP_FLUSH(); return; }
    }
    const unsigned rbase = (4 * lg + q4) * 1024 + p4 * 8;
    if (att) {
        const int aw = wave - 8, gkv = xc >> 1, split = idx & 31;
        unsigned a_q = L_Q + (lj & 7) * 288 + lg * 16, a_k = L_X + (16 * aw + lj) * 256, a_v = L_X + 32768 + (4 * lg + q4) * 256 + ((aw ^ key) << 5) + p4 * 8;
        unsigned a_p = L_P + lane * 8, a_ml = L_ML + (lj & 7) * 80;
        unsigned dst = (((gkv * 4 + (lj >> 1)) * 4 + (aw >> 1)) * 32 + split) * 256 + (aw & 1) * 128 + (lj & 1) * 64 + lg * 16 + WS_S2O;
        unsigned k0 = a_k + ((lg ^ lj) << 4), k1 = a_k + (((4 + lg) ^ lj) << 4), k2 = a_k + (((8 + lg) ^ lj) << 4), k3 = a_k + (((12 + lg) ^ lj) << 4);
        unsigned a_sig = lane == 0 ? L_FLAG : L_JUNK + 4u * lane;
        PIN(a_sig); PIN(a_q); PIN(a_v); PIN(a_p); PIN(a_ml); PIN(dst);
        wait_ge(FL + F_PROG, 96 - D_A);
        STAMP(1);
        v4u kf[4];
        kf[0] = *(LAS v4u*)(L + k0); kf[1] = *(LAS v4u*)(L + k1); kf[2] = *(LAS v4u*)(L + k2); kf[3] = *(LAS v4u*)(L + k3);
        {
            v4u r0, r1;
#pragma nounroll
            for (unsigned sp = 0; sp < GLB_SPIN; ++sp) {
                r0 = LDRAW((lane >> 5) * 16384 + gkv * 4096 + aw * 512 + (lane & 31) * 16, WS_S1) ^ tagw;
                r1 = LDRAW((lane >> 5) * 16384 + gkv * 4096 + aw * 512 + (lane & 31) * 16, WS_S1 + 32768) ^ tagw;
                if (__all(((low(r0) | low(r1)) & TAGM) == 0u)) break;
                CBAR();
            }
            STAMP(2);
            const float q0 = half_sum(val(r0.x) + val(r1.x)), q1 = half_sum(val(r0.y) + val(r1.y));
            const float q2 = half_sum(val(r0.z) + val(r1.z)), q3 = half_sum(val(r0.w) + val(r1.w));
            if (lane < 32) *(LAS v2u*)(L + L_Q + aw * 288 + lane * 8) = v2u{pkbf(q0, q1), pkbf(q2, q3)};
        }
        BARRIER();
        STAMP(3);
        v4u qf[4];
#pragma unroll
        for (int s = 0; s < 4; ++s) qf[s] = *(LAS v4u*)(L + a_q + s * 64);
        __builtin_amdgcn_sched_barrier(0);
        f32x4 sc = {0.f, 0.f, 0.f, 0.f};
#pragma unroll
        for (int s = 0; s < 4; ++s) sc = mfma(kf[s], qf[s], sc);
        __builtin_amdgcn_sched_barrier(0);
        v2u vf[8];
#pragma unroll
        for (int t = 0; t < 8; ++t) vf[t] = trread(L, a_v + t * 4096);
        __builtin_amdgcn_sched_barrier(0);
        {
            const unsigned r01 = pkbf(sc[0], sc[1]), r23 = pkbf(sc[2], sc[3]);
            const float SC = 0.08838834764831845f;
            const unsigned s01 = pkbf(bflo(r01) * SC, bfhi(r01) * SC), s23 = pkbf(bflo(r23) * SC, bfhi(r23) * SC);
            sc[0] = bflo(s01); sc[1] = bfhi(s01); sc[2] = bflo(s23); sc[3] = bfhi(s23);
        }
        const float m = xrow_max(fmx(fmx(sc[0], sc[1]), fmx(sc[2], sc[3])));
#pragma unroll
        for (int r = 0; r < 4; ++r) sc[r] = expdiff(sc[r], m);
        *(LAS v2u*)(L + a_p + aw * 512) = v2u{pkbf(sc[0], sc[1]), pkbf(sc[2], sc[3])};
        const float l = xrow_sum((sc[0] + sc[1]) + (sc[2] + sc[3]));
        if (lane < 8) *(LAS v2u*)(L + a_ml + aw * 8) = v2u{fu(m), fu(l)};
        asm volatile("ds_add_u32 %0, %1 offset:%2" ::"v"(a_sig), "v"(1u), "i"(4 * F_G) : "memory");
        spin_ge_at(L_FLAG + 4 * F_G, 8);
        STAMP(4);
        v2u pf[8];
        f32x4 ml[4];
#pragma unroll
        for (int t = 0; t < 4; ++t) ml[t] = *(LAS f32x4*)(L + a_ml + t * 16);
#pragma unroll
        for (int t = 0; t < 8; ++t) pf[t] = *(LAS v2u*)(L + a_p + t * 512);
        __builtin_amdgcn_sched_barrier(0);
        f32x4 ot[8];
#pragma unroll
        for (int t = 0; t < 8; ++t) ot[t] = mfma16(vf[t], pf[t], f32x4{0.f, 0.f, 0.f, 0.f});
        const float M = fmx(fmx(fmx(ml[0][0], ml[0][2]), fmx(ml[1][0], ml[1][2])), fmx(fmx(ml[2][0], ml[2][2]), fmx(ml[3][0], ml[3][2])));
        f32x4 o = {0.f, 0.f, 0.f, 0.f};
        float lt = 0.f;
#pragma unroll
        for (int t = 0; t < 8; ++t) {
            const float w = expdiff(ml[t >> 1][(t & 1) * 2], M);
            lt = fmaf(w, ml[t >> 1][(t & 1) * 2 + 1], lt);
            o += w * ot[t];
        }
        if (lj < 8) ST(pack4(o[0], o[1], o[2], o[3], tagw), dst);
        if (aw == 0 && lane < 8) ST((v4u{fu(M), tag, fu(lt), tag}), (gkv * 32 + split) * 128 + lj * 16 + WS_S2M);
        STAMP(5);
    } else {
        const int gw = wave - 2;
        unsigned a0 = rbase + (((2 * gw) ^ key) << 5), a1 = rbase + (((2 * gw + 1) ^ key) << 5);
        unsigned dst = ((idx >> 3) * FF + (idx & 7) * 448 + gw * 32 + (lj >> 1) * 16 + lg * 4 + (lj & 1) * 2) * 8 + WS_SH;
        PIN(dst);
        v4u xf[2], af[2][2];
#pragma unroll
        for (int s = 0; s < 2; ++s) xf[s] = xfrag(L, L_XN + 1024 + lg * 8 + s * 64);
        wait_ge(FL + F_PROG, 96);
#pragma unroll
        for (int s = 0; s < 2; ++s) { af[s][0] = trfrag(L, L_X + a0 + s * 32768, 16384); af[s][1] = trfrag(L, L_X + a1 + s * 32768, 16384); }
        SIGNAL(F_GDONE);
        f32x4 g0 = mfma(af[0][0], xf[0], f32x4{0.f, 0.f, 0.f, 0.f}), g1 = mfma(af[0][1], xf[0], f32x4{0.f, 0.f, 0.f, 0.f});
        g0 = mfma(af[1][0], xf[1], g0); g1 = mfma(af[1][1], xf[1], g1);
        wait_ge(FL + F_PROG, 128);
        f32x4 u0 = mfma(trfrag(L, L_Y + a0, 16384), xf[0], f32x4{0.f, 0.f, 0.f, 0.f}), u1 = mfma(trfrag(L, L_Y + a1, 16384), xf[0], f32x4{0.f, 0.f, 0.f, 0.f});
        const bool hi = lj & 1;
        const f32x4 ga = lj < 2 ? g0 : g1;
        float ge = hi ? ga[2] : ga[0], go = hi ? ga[3] : ga[1];
        PIN(ge); PIN(go);
        wait_ge(FL + F_PROG, 160);
        STAMP(2);
        u0 = mfma(trfrag(L, L_Y + 32768 + a0, 16384), xf[1], u0); u1 = mfma(trfrag(L, L_Y + 32768 + a1, 16384), xf[1], u1);
        const f32x4 ua = lj < 2 ? u0 : u1;
        if (lj < 4) ST(pack4(ge, hi ? ua[2] : ua[0], go, hi ? ua[3] : ua[1], tagw), dst);
        STAMP(3);
        if (wave < 8) { STAMP_FLUSH(); return; }
    }
    {
        const int fw = wave - 8;
        const unsigned tb = (att ? L_Y : L_X) + rbase;
        unsigned a[4], dst = (((att ? 0 : 64) + ks) * 1024 + ct2 * 512 + fw * 64 + lj * 16 + lg * 4) * 4 + WS_S3, a_h = L_O + lg * 8;
#pragma unroll
        for (int t = 0; t < 4; ++t) a[t] = tb + (((4 * fw + t) ^ key) << 5);
        PIN(dst); PIN(a_h);
        v4u af[4], ag[4];
        wait_ge(FL + F_PROG, att ? 128 : 192);
#pragma unroll
        for (int t = 0; t < 4; ++t) af[t] = trfrag(L, a[t], 16384);
        BARRIER();
        STAMP(6);
        const v4u h0 = xfrag(L, a_h), h1 = xfrag(L, a_h + 64);
#pragma unroll
        for (int t = 0; t < 4; ++t) ag[t] = trfrag(L, a[t] + 32768, 16384);
        __builtin_amdgcn_sched_barrier(0);
        f32x4 c[4];
#pragma unroll
        for (int t = 0; t < 4; ++t) c[t] = mfma(af[t], h0, f32x4{0.f, 0.f, 0.f, 0.f});
        __builtin_amdgcn_sched_barrier(0);
#pragma unroll
        for (int t = 0; t < 4; ++t) c[t] = mfma(ag[t], h1, c[t]);
        f32x4 cs;
#pragma unroll
        for (int k = 0; k < 4; ++k) {
            float lo = (lj & 1) ? c[1][k] : c[0][k], hi = (lj & 1) ? c[3][k] : c[2][k];
            PIN(lo); PIN(hi);
            cs[k] = (lj & 2) ? hi : lo;
        }
        if (lj < 4) ST(pack4(cs[0], cs[1], cs[2], cs[3], tagw), dst);
        STAMP(7);
    }
    STAMP_FLUSH();
}

extern "C" void kernel_launch(void* const* d_in, const int* in_sizes, int n_in, void* d_out, int out_size, void* d_ws, size_t ws_size,
                              hipStream_t stream) {
    (void)in_sizes; (void)n_in; (void)out_size; (void)ws_size;
    (void)hipFuncSetAttribute((const void*)fused_decode, hipFuncAttributeMaxDynamicSharedMemorySize, (int)LDS_BYTES);
    fused_decode<<<256, 1024, LDS_BYTES, stream>>>((const u16*)d_in[3], (const u16*)d_in[0], (unsigned char*)d_ws, (const u16*)d_in[1],
                                                   (const u16*)d_in[2], (const u16*)d_in[4], (const u16*)d_in[6], (const u16*)d_in[7],
                                                   (const u16*)d_in[8], (u16*)d_out);
}
```

```cpp
#include <hip/hip_runtime.h>
#include <hip/hip_bf16.h>
#include <stdint.h>

#define LAS __attribute__((address_space(3)))
#define GAS __attribute__((address_space(1)))
typedef unsigned short u16;
typedef unsigned v4u __attribute__((ext_vector_type(4)));
typedef unsigned v2u __attribute__((ext_vector_type(2)));
typedef int v4i __attribute__((ext_vector_type(4)));
typedef float f32x4 __attribute__((ext_vector_type(4)));
typedef float f32x2 __attribute__((ext_vector_type(2)));
typedef short s16x4 __attribute__((ext_vector_type(4)));
typedef __bf16 bf16x8 __attribute__((ext_vector_type(8)));
typedef __bf16 bf16x2 __attribute__((ext_vector_type(2)));

constexpr int DM = 1024, SK = 32768, WIN = 4096, FF = 3584;
constexpr unsigned WS_EPOCH = 0;
constexpr unsigned WS_S1 = 4096;
constexpr unsigned WS_SH = WS_S1 + 4 * 4096 * 4;
constexpr unsigned WS_S2O = WS_SH + 16 * 3584 * 8;
constexpr unsigned WS_S2M = WS_S2O + 64 * 32 * 256;
constexpr unsigned WS_S3 = WS_S2M + 32 * 32 * 16;
constexpr unsigned WS_END = WS_S3 + 128 * 1024 * 4;
constexpr unsigned L_X = 0;
constexpr unsigned L_Y = 65536;
constexpr unsigned L_XN = 131072;
constexpr unsigned L_Q = L_XN + 2048;
constexpr unsigned L_P = L_Q + 2304;
constexpr unsigned L_ML = L_P + 4096;
constexpr unsigned L_O = L_ML + 1024;
constexpr unsigned L_FLAG = L_O + 128;
constexpr unsigned L_JUNK = L_FLAG + 64;
constexpr unsigned LDS_BYTES = L_JUNK + 1024;
enum { F_PROG = 0, F_P = 1, F_TAG = 2, F_WQDONE = 3, F_GDONE = 4, F_XN = 5 };

constexpr int D_Q = 28;  constexpr int D_A = 24;  constexpr int D_W = 20;  constexpr int D_B = 32;  constexpr int D_D = 24;
constexpr int SH_SLEEP = 8;  constexpr int S2_SLEEP = 68;  constexpr int S3_SLEEP = 26;
constexpr unsigned LDS_SPIN = 1u << 18, GLB_SPIN = 1u << 16;
constexpr int AUX_SC1 = 16;
constexpr float LOG2E = 1.4426950408889634f;

__device__ __forceinline__ float bflo(unsigned u) { return __builtin_bit_cast(float, u << 16); }
__device__ __forceinline__ float bfhi(unsigned u) { return __builtin_bit_cast(float, u & 0xffff0000u); }
__device__ __forceinline__ float rbf(float f) { return (float)(__bf16)f; }
__device__ __forceinline__ unsigned pkbf(float lo, float hi) { bf16x2 v = {(__bf16)lo, (__bf16)hi}; return __builtin_bit_cast(unsigned, v); }
__device__ __forceinline__ unsigned fu(float f) { return __builtin_bit_cast(unsigned, f); }
__device__ __forceinline__ float uf(unsigned u) { return __builtin_bit_cast(float, u); }
__device__ __forceinline__ float fmx(float a, float b) { return __builtin_elementwise_maximum(a, b); }
__device__ __forceinline__ float expdiff(float a, float mx) { return __builtin_amdgcn_exp2f((a - mx) * LOG2E); }
constexpr unsigned TAGM = 0x3ffu;
__device__ __forceinline__ unsigned tw(float a, unsigned tagw) { return (fu(a) & ~TAGM) | tagw; }
__device__ __forceinline__ v4u pack4(float a, float b, float c, float d, unsigned tagw) { return v4u{tw(a, tagw), tw(b, tagw), tw(c, tagw), tw(d, tagw)}; }
__device__ __forceinline__ float val(unsigned w) { return uf(w); }
__device__ __forceinline__ unsigned low(v4u r) { return (r.x | r.y | r.z) | r.w; }
__device__ __forceinline__ bool fresh(v4u r) { return (low(r) & TAGM) == 0u; }

template <int CTRL> __device__ __forceinline__ float dppm(float v) {
    return __builtin_bit_cast(float, __builtin_amdgcn_mov_dpp(__builtin_bit_cast(int, v), CTRL, 0xf, 0xf, true));
}
__device__ __forceinline__ float row_sum(float v) {
    v += dppm<0xB1>(v); v += dppm<0x4E>(v); v += dppm<0x141>(v); v += dppm<0x128>(v);
    return v;
}
__device__ __forceinline__ float half_sum(float x) {
    auto t = __builtin_amdgcn_permlane32_swap(fu(x), fu(x), false, false);
    return uf(t[0]) + uf(t[1]);
}
__device__ __forceinline__ float xrow_max(float x) {
    auto s = __builtin_amdgcn_permlane16_swap(fu(x), fu(x), false, false);
    x = fmx(uf(s[0]), uf(s[1]));
    auto t = __builtin_amdgcn_permlane32_swap(fu(x), fu(x), false, false);
    return fmx(uf(t[0]), uf(t[1]));
}
__device__ __forceinline__ float xrow_sum(float x) {
    auto s = __builtin_amdgcn_permlane16_swap(fu(x), fu(x), false, false);
    return half_sum(uf(s[0]) + uf(s[1]));
}
__device__ __forceinline__ float wave_sum(float v) { return xrow_sum(row_sum(v)); }
__device__ __forceinline__ f32x4 mfma(v4u a, v4u b, f32x4 c) {
    return __builtin_amdgcn_mfma_f32_16x16x32_bf16(__builtin_bit_cast(bf16x8, a), __builtin_bit_cast(bf16x8, b), c, 0, 0, 0);
}
__device__ __forceinline__ f32x4 mfma16(v2u a, v2u b, f32x4 c) {
    return __builtin_amdgcn_mfma_f32_16x16x16bf16_1k(__builtin_bit_cast(s16x4, a), __builtin_bit_cast(s16x4, b), c, 0, 0, 0);
}
__device__ __forceinline__ v2u trread(LAS unsigned char* L, unsigned off) {
    return __builtin_bit_cast(v2u, __builtin_amdgcn_ds_read_tr16_b64_v4i16((LAS s16x4*)(L + off)));
}
__device__ __forceinline__ v4u trfrag(LAS unsigned char* L, unsigned off, unsigned toff) {
    const v2u a = trread(L, off), b = trread(L, off + toff);
    return v4u{a.x, a.y, b.x, b.y};
}
__device__ __forceinline__ v4u xfrag(LAS unsigned char* L, unsigned off) {
    const v2u a = *(LAS v2u*)(L + off), b = *(LAS v2u*)(L + off + 32);
    return v4u{a.x, a.y, b.x, b.y};
}
#define CBAR() asm volatile("" ::: "memory")
#define BARRIER() asm volatile("s_waitcnt lgkmcnt(0)\n\ts_barrier" ::: "memory")
#define PIN(v) asm volatile("" : "+v"(v))
__device__ __forceinline__ void wait_ge_at(unsigned addr, unsigned want) {
    unsigned v, sv, n = LDS_SPIN;
    asm volatile("s_branch 1f\n"
                 "0:\n\t"
                 "s_sleep 1\n"
                 "1:\n\t"
                 "ds_read_b32 %0, %3\n\t"
                 "s_sub_u32 %2, %2, 1\n\t"
                 "s_cbranch_scc1 2f\n\t"
                 "s_waitcnt lgkmcnt(0)\n\t"
                 "v_readfirstlane_b32 %1, %0\n\t"
                 "s_cmp_lt_u32 %1, %4\n\t"
                 "s_cbranch_scc1 0b\n"
                 "2:\n\t"
                 "s_waitcnt lgkmcnt(0)"
                 : "=&v"(v), "=&s"(sv), "+&s"(n) : "v"(addr), "s"(want) : "memory", "scc");
}
__device__ __forceinline__ void spin_ge_at(unsigned addr, unsigned want) {
    unsigned v, sv, n = LDS_SPIN;
    asm volatile("1:\n\t"
                 "ds_read_b32 %0, %3\n\t"
                 "s_sub_u32 %2, %2, 1\n\t"
                 "s_cbranch_scc1 2f\n\t"
                 "s_waitcnt lgkmcnt(0)\n\t"
                 "v_readfirstlane_b32 %1, %0\n\t"
                 "s_cmp_lt_u32 %1, %4\n\t"
                 "s_cbranch_scc1 1b\n"
                 "2:\n\t"
                 "s_waitcnt lgkmcnt(0)"
                 : "=&v"(v), "=&s"(sv), "+&s"(n) : "v"(addr), "s"(want) : "memory", "scc");
}
#define wait_ge(ptr, want) wait_ge_at(L_FLAG + 4u * (unsigned)((ptr) - FL), (want))
#define SIGNAL(flag) asm volatile("s_waitcnt lgkmcnt(0)\n\tds_add_u32 %0, %1 offset:%2" ::"v"(lane == 0 ? L_FLAG : L_JUNK + 4u * lane), "v"(1u), "i"(4 * (flag)) : "memory")
__device__ __forceinline__ v4i mkrsrc(const void* p, unsigned bytes) {
    const uint64_t a = (uint64_t)p;
    return v4i{(int)(unsigned)a, (int)((unsigned)(a >> 32) & 0xffffu), (int)bytes, 0x00020000};
}
#define VMWAIT(n) asm volatile("s_waitcnt vmcnt(%0)" ::"i"(n) : "memory")
#define LDRAW(voff, soff) __builtin_amdgcn_raw_buffer_load_b128(wsr, (voff), (soff), AUX_SC1)
#define ST(v, voff) __builtin_amdgcn_raw_buffer_store_b128((v), wsr, (voff), 0, AUX_SC1)

template <int N, unsigned BASE, unsigned STEP>
__device__ __forceinline__ void gather(__amdgpu_buffer_rsrc_t wsr, unsigned off, unsigned tagw, v4u (&r)[N]) {
#pragma unroll
    for (int i = 0; i < N; ++i) r[i] = LDRAW(off, BASE + i * STEP);
    unsigned bad = 0;
#pragma unroll
    for (int i = 0; i < N; ++i) { r[i] ^= tagw; bad |= low(r[i]); }
    if (__builtin_expect(__all((bad & TAGM) == 0u), 1)) return;
    unsigned pend = 0;
#pragma unroll
    for (int i = 0; i < N; ++i) pend |= __all(fresh(r[i])) ? 0u : 1u << i;
#pragma nounroll
    for (unsigned sp = 0; sp < GLB_SPIN && pend; ++sp) {
        CBAR();
#pragma unroll
        for (int i = 0; i < N; ++i) if (pend & (1u << i)) r[i] = LDRAW(off, BASE + i * STEP);
        VMWAIT(0);
#pragma unroll
        for (int i = 0; i < N; ++i) if (pend & (1u << i)) { r[i] ^= tagw; if (__all(fresh(r[i]))) pend &= ~(1u << i); }
    }
}

template <int DEP>
__device__ __forceinline__ void dma4(v4i rs, v4u v, unsigned& soff, unsigned sstep, unsigned lds) {
    asm volatile("s_mov_b32 m0, %[lds]\n\t"
                 "s_nop 0\n\t"
                 "buffer_load_dwordx4 %[v0], %[rs], %[so] offen nt lds\n\t"
                 "s_add_u32 m0, m0, 0x400\n\t"
                 "s_add_u32 %[so], %[so], %[st]\n\t"
                 "buffer_load_dwordx4 %[v1], %[rs], %[so] offen nt lds\n\t"
                 "s_add_u32 m0, m0, 0x400\n\t"
                 "s_add_u32 %[so], %[so], %[st]\n\t"
                 "s_waitcnt vmcnt(%[dep])\n\t"
                 "buffer_load_dwordx4 %[v2], %[rs], %[so] offen nt lds\n\t"
                 "s_add_u32 m0, m0, 0x400\n\t"
                 "s_add_u32 %[so], %[so], %[st]\n\t"
                 "buffer_load_dwordx4 %[v3], %[rs], %[so] offen nt lds\n\t"
                 "s_add_u32 %[so], %[so], %[st]\n\t"
                 "s_waitcnt vmcnt(%[dep])"
                 : [so] "+&s"(soff)
                 : [lds] "s"(lds), [v0] "v"(v.x), [v1] "v"(v.y), [v2] "v"(v.z), [v3] "v"(v.w), [rs] "s"(rs), [st] "s"(sstep), [dep] "i"(DEP)
                 : "memory", "scc");
}
template <int DEP>
__device__ __forceinline__ void stream(v4i rs, v4u va, v4u vb, unsigned soff, unsigned sstep, unsigned lds, int ng, int& prog, unsigned progaddr) {
    const unsigned end = lds + ng * 4096;
#pragma nounroll
    do {
        dma4<DEP>(rs, va, soff, sstep, lds);
        prog += 4;
        asm volatile("ds_max_i32 %0, %1" ::"v"(progaddr), "v"(prog - DEP) : "memory");
        dma4<DEP>(rs, vb, soff, sstep, lds + 4096);
        prog += 4; lds += 8192;
        asm volatile("ds_max_i32 %0, %1" ::"v"(progaddr), "v"(prog - DEP) : "memory");
    } while (lds != end);
}

__global__ void __launch_bounds__(1024, 4)
fused_decode(const u16* __restrict__ wq, const u16* __restrict__ x, unsigned char* ws, const u16* __restrict__ kc, const u16* __restrict__ vc,
             const u16* __restrict__ wo, const u16* __restrict__ wg, const u16* __restrict__ wu, const u16* __restrict__ wd, u16* out) {
    extern __shared__ __attribute__((aligned(16))) unsigned char smem[];
    LAS unsigned char* L = (LAS unsigned char*)smem;
    volatile LAS unsigned* FL = (volatile LAS unsigned*)(L + L_FLAG);
    const int tid = threadIdx.x, lane = tid & 63, wave = __builtin_amdgcn_readfirstlane(tid >> 6), b = blockIdx.x;
    FL[lane & 15] = 0u;
    v4u xa, xb;
    v2u xs, xg;
    unsigned ep;
    {
        const int rl = lane >> 3, p = lane & 7;
        const unsigned v0 = rl * 8192 + (((p >> 1) ^ ((rl >> 1) & 3)) << 5) + (p & 1) * 16;
        unsigned so = (unsigned)(b >> 6) * 256 * 8192 + (b & 63) * 128;
        const v4i rs = mkrsrc(wq, DM * 4096 * 2);
        const unsigned l16 = lane * 16, zero = 0;
        const unsigned os = (b >> 6) * 512 + lane * 8, og = ((b & 7) * 2 + (b >> 7)) * 128 + (lane & 15) * 8;
        uint64_t pc;
        unsigned coff = (b >> 3) * 1024 + l16;
        asm volatile("s_cmp_lg_u32 %[wave], 0\n\t"
                     "s_cbranch_scc1 1f\n\t"
                     "s_mov_b32 m0, %[lds]\n"
                     "0:\n\t"
                     "s_nop 0\n\t"
                     "buffer_load_dwordx4 %[v0], %[rs], %[so] offen nt lds\n\t"
                     "s_add_u32 m0, m0, 0x400\n\t"
                     "s_add_u32 %[so], %[so], 0x10000\n\t"
                     "buffer_load_dwordx4 %[v0], %[rs], %[so] offen nt lds\n\t"
                     "s_add_u32 m0, m0, 0x400\n\t"
                     "s_add_u32 %[so], %[so], 0x10000\n\t"
                     "buffer_load_dwordx4 %[v0], %[rs], %[so] offen nt lds\n\t"
                     "s_add_u32 m0, m0, 0x400\n\t"
                     "s_add_u32 %[so], %[so], 0x10000\n\t"
                     "buffer_load_dwordx4 %[v0], %[rs], %[so] offen nt lds\n\t"
                     "s_add_u32 m0, m0, 0x400\n\t"
                     "s_add_u32 %[so], %[so], 0x10000\n\t"
                     "s_cmp_lg_u32 m0, %[lend]\n\t"
                     "s_cbranch_scc1 0b\n\t"
                     "s_branch 3f\n"
                     "1:\n\t"
                     "s_cmp_lg_u32 %[wave], 1\n\t"
                     "s_cbranch_scc1 2f\n\t"
                     "global_load_dwordx4 %[xa], %[l16], %[x]\n\t"
                     "global_load_dwordx4 %[xb], %[l16], %[x] offset:1024\n\t"
                     "global_load_dwordx2 %[xs], %[os], %[x]\n\t"
                     "global_load_dwordx2 %[xg], %[og], %[x]\n\t"
                     "global_load_dword %[ep], %[zero], %[ws] sc1\n\t"
                     "s_branch 3f\n"
                     "2:\n\t"
                     "s_cmp_lg_u32 %[wave], 2\n\t"
                     "s_cbranch_scc1 3f\n\t"
                     "s_mov_b32 m0, %[junk]\n\t"
                     ".subsection 1\n"
                     "5:\n\t"
                     ".subsection 0\n\t"
                     "v_min_u32 %[coff], 5b-4f-16, %[coff]\n\t"
                     "s_getpc_b64 %[pc]\n"
                     "4:\n\t"
                     "global_load_lds_dwordx4 %[coff], %[pc]\n"
                     "3:\n\t"
                     "s_waitcnt lgkmcnt(0)\n\t"
                     "s_barrier"
                     : [so] "+&s"(so), [xa] "=&v"(xa), [xb] "=&v"(xb), [xs] "=&v"(xs), [xg] "=&v"(xg), [ep] "=&v"(ep), [pc] "=&s"(pc), [coff] "+&v"(coff)
                     : [wave] "s"(wave), [lds] "i"(L_Y), [lend] "i"(L_Y + 24576), [junk] "i"(L_JUNK), [v0] "v"(v0), [rs] "s"(rs), [l16] "v"(l16), [zero] "v"(zero), [os] "v"(os), [og] "v"(og), [x] "s"(x), [ws] "s"(ws)
                       , "s"(kc), "s"(vc), "s"(wo), "s"(wg), "s"(wu), "s"(wd), "s"(out)
                     : "memory", "scc");
        const u16 *p1 = (b & 8) ? kc : wg, *p2 = (b & 8) ? vc : wu, *p3 = (b & 8) ? wo : wd;
        if (wave == 0) {
            const int xc = b & 7, bi = b >> 3, idx = xc * 16 + (bi >> 1), ks = idx >> 1, ct2 = idx & 1;
            const int lj = lane & 15, lg = lane >> 4;
            const unsigned pa = lane == 0 ? L_FLAG + 4 * F_PROG : L_JUNK + 4 * lane;
            int cnt = 24;
            PIN(cnt);
            stream<D_Q>(rs, v4u{v0, v0, v0, v0}, v4u{v0, v0, v0, v0}, so, 65536, L_Y + 24576, 2, cnt, pa);
            v4u va, vb;
#pragma unroll
            for (int i = 0; i < 4; ++i) { va[i] = l16 ^ (i << 5); vb[i] = l16 ^ ((i + 4) << 5); }
            if (bi & 1) {
                const size_t kvoff = ((size_t)(xc >> 1) * SK + (SK - WIN) + (idx & 31) * 128) * 128;
                const unsigned k0 = lg * 256 + ((lj ^ lg) << 4);
                const v4u kv = v4u{k0, k0 ^ 0x40u, k0 ^ 0x80u, k0 ^ 0xC0u};
                stream<D_A>(mkrsrc(p1 + kvoff, 32768), kv, kv, 0, 1024, L_X, 8, cnt, pa);
                const unsigned u0 = lg * 256 + (((lj >> 1) ^ lg) << 5) + (lj & 1) * 16;
                const v4u vv = v4u{u0, u0 ^ 0x80u, u0, u0 ^ 0x80u};
                stream<D_A>(mkrsrc(p2 + kvoff, 32768), vv, vv, 0, 1024, L_X + 32768, 8, cnt, pa);
                const v4i wr = mkrsrc(p3, 4096 * DM * 2);
                const unsigned so2 = (unsigned)((ks >> 2) * 256 + (ks & 3) * 32) * 2048 + ct2 * 1024;
                VMWAIT(0);
                BARRIER();
                stream<D_W>(wr, va, vb, so2, 2048, L_Y, 8, cnt, pa);
                stream<D_W>(wr, va, vb, so2 + 128 * 2048, 2048, L_Y + 32768, 8, cnt, pa);
            } else {
                v4u ga, gb;
#pragma unroll
                for (int i = 0; i < 4; ++i) { ga[i] = va[i] < 896 ? va[i] : 0x40000000u; gb[i] = vb[i] < 896 ? vb[i] : 0x40000000u; }
                const unsigned so2 = (unsigned)(idx >> 3) * 64 * 7168 + (idx & 7) * 896;
                stream<D_B>(mkrsrc(p1, DM * FF * 2), ga, gb, so2, 7168, L_X, 16, cnt, pa);
                wait_ge(FL + F_WQDONE, 2);
                stream<D_B>(mkrsrc(p2, DM * FF * 2), ga, gb, so2, 7168, L_Y, 16, cnt, pa);
                wait_ge(FL + F_GDONE, 14);
                const v4i dr = mkrsrc(p3, FF * DM * 2);
                const unsigned so3 = (unsigned)ks * 56 * 2048 + ct2 * 1024;
                stream<D_D>(dr, va, vb, so3, 2048, L_X, 14, cnt, pa);
                VMWAIT(24);
                asm volatile("ds_max_i32 %0, %1" ::"v"(pa), "v"(cnt - 24) : "memory");
            }
            VMWAIT(0);
            asm volatile("s_barrier" ::: "memory");
            return;
        }
    }
    const int xc = b & 7, bi = b >> 3;
    const bool att = bi & 1;
    const int idx = xc * 16 + (bi >> 1);
    const int ks = idx >> 1, ct2 = idx & 1;
    const int lj = lane & 15, lg = lane >> 4;
    const int q4 = lj >> 2, p4 = lane & 3;
    const unsigned key = (unsigned)(q4 | ((lg & 1) << 2));
    const __amdgpu_buffer_rsrc_t wsr = __builtin_amdgcn_make_buffer_rsrc((void*)ws, 0, (int)WS_END, 0x00020000);

    if (wave == 1) {
        asm volatile("s_waitcnt vmcnt(0)" : "+v"(xa), "+v"(xb), "+v"(xs), "+v"(xg), "+v"(ep)::"memory");
        const float c0 = bflo(__builtin_amdgcn_readfirstlane(xa.x));
        f32x2 s1 = {0.f, 0.f}, s2 = {0.f, 0.f};
#pragma unroll
        for (int i = 0; i < 4; ++i) {
            const f32x2 da = f32x2{bflo(xa[i]), bfhi(xa[i])} - c0, db = f32x2{bflo(xb[i]), bfhi(xb[i])} - c0;
            s1 += da + db;
            s2 += da * da; s2 += db * db;
        }
        const float md = wave_sum(s1[0] + s1[1]) * (1.0f / DM), e2 = wave_sum(s2[0] + s2[1]) * (1.0f / DM);
        const float var = fmx(fmaf(-md, md, e2), 0.f);
        const float mb = rbf(c0 + md), den = rbf(__builtin_amdgcn_sqrtf(rbf(rbf(var) + 1e-5f)));
        const float rden = __builtin_amdgcn_rcpf(den);
        v2u oq, og2;
#pragma unroll
        for (int i = 0; i < 2; ++i) {
            unsigned dq = pkbf(bflo(xs[i]) - mb, bfhi(xs[i]) - mb), dg = pkbf(bflo(xg[i]) - mb, bfhi(xg[i]) - mb);
            PIN(dq); PIN(dg);
            oq[i] = pkbf(bflo(dq) * rden, bfhi(dq) * rden);
            og2[i] = pkbf(bflo(dg) * rden, bfhi(dg) * rden);
        }
        *(LAS v2u*)(L + L_XN + lane * 8) = oq;
        *(LAS v2u*)(L + L_XN + 1024 + lane * 8) = og2;
        const unsigned tag = ep + 1u, tagw = tag & TAGM;
        FL[F_TAG] = tag;
        SIGNAL(F_XN);
        const int own = xc * 32 + bi, col = own * 4 + ((lg & 1) << 1) + (lg >> 1);
        unsigned o_s3 = lane * 4096 + own * 16;
        const float xres = bflo(x[col]);
        PIN(o_s3);
        u16* outp = out + col;
        asm volatile("" : "+v"(outp));
        if (att) {
            unsigned a_o = L_O + (lj & 4) * 16 + (lj >> 3) * 32 + (lj & 3) * 8 + ((lg & 1) << 2) + (lg & 2);
            PIN(a_o);
            BARRIER();
            __builtin_amdgcn_s_sleep(S2_SLEEP);
            const unsigned o_ml = ((ks >> 4) * 32 + (lane & 31)) * 128 + (((ks >> 2) & 3) * 2 + (lane >> 5)) * 16, o_r = ks * 8192 + lane * 16;
            v4u r[8], mlv = LDRAW(o_ml, WS_S2M);
            gather<8, WS_S2O, 1024>(wsr, o_r, tagw, r);
#pragma nounroll
            for (unsigned sp = 0; sp < GLB_SPIN; ++sp) {
                if (__builtin_expect(__all(((mlv.y ^ tag) | (mlv.w ^ tag)) == 0u), 1)) break;
                CBAR();
                mlv = LDRAW(o_ml, WS_S2M);
            }
            float Mx = uf(mlv.x);
            Mx = fmx(Mx, dppm<0xB1>(Mx)); Mx = fmx(Mx, dppm<0x4E>(Mx)); Mx = fmx(Mx, dppm<0x141>(Mx)); Mx = fmx(Mx, dppm<0x128>(Mx));
            { auto t = __builtin_amdgcn_permlane16_swap(fu(Mx), fu(Mx), false, false); Mx = fmx(uf(t[0]), uf(t[1])); }
            const float wgt = expdiff(uf(mlv.x), Mx);
            float w[8];
#pragma unroll
            for (int i = 0; i < 8; ++i) w[i] = uf(__builtin_amdgcn_ds_bpermute((lg + (lj & 4) * 8) * 4 + 16 * i, fu(wgt)));
            float Ls = row_sum(wgt * uf(mlv.z));
            { auto t = __builtin_amdgcn_permlane16_swap(fu(Ls), fu(Ls), false, false); Ls = uf(t[0]) + uf(t[1]); }
            Ls = __builtin_amdgcn_rcpf(Ls);
            const float rl = (lj & 4) ? uf(__builtin_amdgcn_readlane(fu(Ls), 32)) : uf(__builtin_amdgcn_readlane(fu(Ls), 0));
            f32x2 a01 = {0.f, 0.f}, a23 = {0.f, 0.f};
#pragma unroll
            for (int i = 0; i < 8; ++i) {
                a01 += w[i] * f32x2{val(r[i].x), val(r[i].y)};
                a23 += w[i] * f32x2{val(r[i].z), val(r[i].w)};
            }
            auto t01 = __builtin_amdgcn_permlane32_swap(fu(a01[0]), fu(a01[1]), false, false);
            auto t23 = __builtin_amdgcn_permlane32_swap(fu(a23[0]), fu(a23[1]), false, false);
            auto tt = __builtin_amdgcn_permlane16_swap(fu(uf(t01[0]) + uf(t01[1])), fu(uf(t23[0]) + uf(t23[1])), false, false);
            *(LAS __bf16*)(L + a_o) = (__bf16)((uf(tt[0]) + uf(tt[1])) * rl);
        } else {
            wait_ge(FL + F_PROG, 192);
            __builtin_amdgcn_s_sleep(SH_SLEEP);
            const int un = lane & 31;
            const unsigned off = ((lane >> 5) * FF + ks * 56 + (un < 28 ? un : 27) * 2) * 8;
            v4u r[8];
            gather<8, WS_SH, 2 * FF * 8>(wsr, off, tagw, r);
            f32x2 c0 = {0.f, 0.f}, c1 = {0.f, 0.f};
#pragma unroll
            for (int i = 0; i < 8; i += 2) {
                c0 += f32x2{val(r[i].x), val(r[i].y)} + f32x2{val(r[i + 1].x), val(r[i + 1].y)};
                c1 += f32x2{val(r[i].z), val(r[i].w)} + f32x2{val(r[i + 1].z), val(r[i + 1].w)};
            }
            auto tg = __builtin_amdgcn_permlane32_swap(fu(c0[0]), fu(c1[0]), false, false);
            auto tu = __builtin_amdgcn_permlane32_swap(fu(c0[1]), fu(c1[1]), false, false);
            const unsigned gu = pkbf(uf(tg[0]) + uf(tg[1]), uf(tu[0]) + uf(tu[1]));
            const float g = bflo(gu);
            const float hv = rbf(g * __builtin_amdgcn_rcpf(1.0f + __builtin_amdgcn_exp2f(-g * LOG2E))) * bfhi(gu);
            *(LAS __bf16*)(L + L_O + un * 4 + (lane >> 5) * 2) = (__bf16)(un < 28 ? hv : 0.f);
        }
        BARRIER();
        __builtin_amdgcn_s_sleep(S3_SLEEP);
        v4u w0, w1;
        unsigned x0, x1, x2, x3, y0, y1, y2, y3;
        {
            const v4i wr4 = mkrsrc(ws, WS_END);
            unsigned n = GLB_SPIN, t0, t1;
            uint64_t m1, m2;
            asm volatile("s_mov_b64 vcc, -1\n\t"
                         "s_mov_b64 %[m1], -1\n"
                         "0:\n\t"
                         "s_mov_b64 exec, vcc\n\t"
                         "s_cbranch_execz 2f\n\t"
                         "buffer_load_dwordx4 %[w0], %[off], %[rs], %[b0] offen sc1\n"
                         "2:\n\t"
                         "s_mov_b64 exec, %[m1]\n\t"
                         "s_cbranch_execz 3f\n\t"
                         "buffer_load_dwordx4 %[w1], %[off], %[rs], %[b1] offen sc1\n"
                         "3:\n\t"
                         "s_mov_b64 exec, -1\n\t"
                         "s_sub_u32 %[n], %[n], 1\n\t"
                         "s_waitcnt vmcnt(0)\n\t"
                         "s_cbranch_scc1 1f\n\t"
                         "v_xor_b32 %[x0], v100, %[tg]\n\t"
                         "v_xor_b32 %[x1], v101, %[tg]\n\t"
                         "v_xor_b32 %[x2], v102, %[tg]\n\t"
                         "v_xor_b32 %[x3], v103, %[tg]\n\t"
                         "v_xor_b32 %[y0], v104, %[tg]\n\t"
                         "v_xor_b32 %[y1], v105, %[tg]\n\t"
                         "v_xor_b32 %[y2], v106, %[tg]\n\t"
                         "v_xor_b32 %[y3], v107, %[tg]\n\t"
                         "v_or3_b32 %[t0], %[x0], %[x1], %[x2]\n\t"
                         "v_or3_b32 %[t1], %[y0], %[y1], %[y2]\n\t"
                         "v_bitop3_b32 %[t0], %[t0], %[mk], %[x3] bitop3:0xc8\n\t"
                         "v_bitop3_b32 %[t1], %[t1], %[mk], %[y3] bitop3:0xc8\n\t"
                         "v_cmp_ne_u32 vcc, 0, %[t0]\n\t"
                         "v_cmp_ne_u32 %[m1], 0, %[t1]\n\t"
                         "s_or_b64 %[m2], vcc, %[m1]\n\t"
                         "s_cbranch_scc1 0b\n"
                         "1:"
                         : [w0] "=&{v[100:103]}"(w0), [w1] "=&{v[104:107]}"(w1), [x0] "=&v"(x0), [x1] "=&v"(x1), [x2] "=&v"(x2), [x3] "=&v"(x3),
                           [y0] "=&v"(y0), [y1] "=&v"(y1), [y2] "=&v"(y2), [y3] "=&v"(y3), [t0] "=&v"(t0), [t1] "=&v"(t1), [m1] "=&s"(m1), [m2] "=&s"(m2), [n] "+&s"(n)
                         : [off] "v"(o_s3), [rs] "s"(wr4), [b0] "s"(WS_S3), [b1] "s"(WS_S3 + 64 * 4096), [tg] "v"(tagw), [mk] "s"(TAGM)
                         : "memory", "scc", "vcc");
        }
        auto t01 = __builtin_amdgcn_permlane32_swap(fu(val(x0) + val(y0)), fu(val(x1) + val(y1)), false, false);
        auto t23 = __builtin_amdgcn_permlane32_swap(fu(val(x2) + val(y2)), fu(val(x3) + val(y3)), false, false);
        auto t = __builtin_amdgcn_permlane16_swap(fu(uf(t01[0]) + uf(t01[1])), fu(uf(t23[0]) + uf(t23[1])), false, false);
        const float tot = row_sum(uf(t[0]) + uf(t[1])) + xres;
        if (lj == 0) {
            *(GAS __bf16*)outp = (__bf16)tot;
            if (b == 0 && lane == 0) __hip_atomic_store((GAS unsigned*)(ws + WS_EPOCH), tag, __ATOMIC_RELAXED, __HIP_MEMORY_SCOPE_AGENT);
        }
        return;
    }
    if (att && wave < 6) return;
    wait_ge(FL + F_XN, 1);
    const unsigned tag = FL[F_TAG], tagw = tag & TAGM;
    if ((wave & 14) == 6) {
        const int c = wave & 1;
        const unsigned row = 4 * lg + q4;
        const unsigned a0 = L_Y + row * 128 + (((2 * c) ^ ((row >> 1) & 3)) << 5) + p4 * 8, a1 = a0 ^ 32u;
        unsigned dst = ((b >> 6) * 4096 + (b & 63) * 64 + c * 32 + lj * 16 + lg * 4) * 4 + WS_S1;
        PIN(dst);
        f32x4 acc0 = {0.f, 0.f, 0.f, 0.f}, acc1 = {0.f, 0.f, 0.f, 0.f};
        v4u xq[8], af[6], ag[6];
#pragma unroll
        for (int s = 0; s < 8; ++s) xq[s] = xfrag(L, L_XN + lg * 8 + s * 64);
        wait_ge(FL + F_PROG, 24);
#pragma unroll
        for (int s = 0; s < 6; ++s) { af[s] = trfrag(L, a0 + s * 4096, 2048); ag[s] = trfrag(L, a1 + s * 4096, 2048); }
        __builtin_amdgcn_sched_barrier(0);
#pragma unroll
        for (int s = 0; s < 6; ++s) { acc0 = mfma(af[s], xq[s], acc0); acc1 = mfma(ag[s], xq[s], acc1); }
        __builtin_amdgcn_sched_barrier(0);
        wait_ge(FL + F_PROG, 32);
#pragma unroll
        for (int s = 0; s < 2; ++s) { af[s] = trfrag(L, a0 + (6 + s) * 4096, 2048); ag[s] = trfrag(L, a1 + (6 + s) * 4096, 2048); }
        __builtin_amdgcn_sched_barrier(0);
#pragma unroll
        for (int s = 0; s < 2; ++s) { acc0 = mfma(af[s], xq[6 + s], acc0); acc1 = mfma(ag[s], xq[6 + s], acc1); }
        const f32x4 acc = lj == 0 ? acc0 : acc1;
        if (lj < 2) ST(pack4(uf(fu(acc[0]) + 0x200u), uf(fu(acc[1]) + 0x200u), uf(fu(acc[2]) + 0x200u), uf(fu(acc[3]) + 0x200u), tagw), dst);
        SIGNAL(F_WQDONE);
        if (att) return;
    }
    const unsigned rbase = (4 * lg + q4) * 1024 + p4 * 8;
    if (att) {
        const int aw = wave - 8, gkv = xc >> 1, split = idx & 31;
        unsigned a_q = L_Q + (lj & 7) * 288 + lg * 16, a_k = L_X + (16 * aw + lj) * 256, a_v = L_X + 32768 + (4 * lg + q4) * 256 + ((aw ^ key) << 5) + p4 * 8;
        unsigned a_p = L_P + lane * 8, a_ml = L_ML + (lj & 7) * 80;
        unsigned dst = (((gkv * 4 + (lj >> 1)) * 4 + (aw >> 1)) * 32 + split) * 256 + (aw & 1) * 128 + (lj & 1) * 64 + lg * 16 + WS_S2O;
        unsigned k0 = a_k + ((lg ^ lj) << 4), k1 = a_k + (((4 + lg) ^ lj) << 4), k2 = a_k + (((8 + lg) ^ lj) << 4), k3 = a_k + (((12 + lg) ^ lj) << 4);
        unsigned a_sig = lane == 0 ? L_FLAG : L_JUNK + 4u * lane;
        PIN(a_sig); PIN(a_q); PIN(a_v); PIN(a_p); PIN(a_ml); PIN(dst);
        wait_ge(FL + F_PROG, 96 - D_A);
        v4u kf[4];
        kf[0] = *(LAS v4u*)(L + k0); kf[1] = *(LAS v4u*)(L + k1); kf[2] = *(LAS v4u*)(L + k2); kf[3] = *(LAS v4u*)(L + k3);
        {
            v4u r0, r1;
#pragma nounroll
            for (unsigned sp = 0; sp < GLB_SPIN; ++sp) {
                r0 = LDRAW((lane >> 5) * 16384 + gkv * 4096 + aw * 512 + (lane & 31) * 16, WS_S1) ^ tagw;
                r1 = LDRAW((lane >> 5) * 16384 + gkv * 4096 + aw * 512 + (lane & 31) * 16, WS_S1 + 32768) ^ tagw;
                if (__all(((low(r0) | low(r1)) & TAGM) == 0u)) break;
                CBAR();
            }
            const float q0 = half_sum(val(r0.x) + val(r1.x)), q1 = half_sum(val(r0.y) + val(r1.y));
            const float q2 = half_sum(val(r0.z) + val(r1.z)), q3 = half_sum(val(r0.w) + val(r1.w));
            if (lane < 32) *(LAS v2u*)(L + L_Q + aw * 288 + lane * 8) = v2u{pkbf(q0, q1), pkbf(q2, q3)};
        }
        BARRIER();
        v4u qf[4];
#pragma unroll
        for (int s = 0; s < 4; ++s) qf[s] = *(LAS v4u*)(L + a_q + s * 64);
        __builtin_amdgcn_sched_barrier(0);
        f32x4 sc = {0.f, 0.f, 0.f, 0.f};
#pragma unroll
        for (int s = 0; s < 4; ++s) sc = mfma(kf[s], qf[s], sc);
        __builtin_amdgcn_sched_barrier(0);
        v2u vf[8];
#pragma unroll
        for (int t = 0; t < 8; ++t) vf[t] = trread(L, a_v + t * 4096);
        __builtin_amdgcn_sched_barrier(0);
        {
            const unsigned r01 = pkbf(sc[0], sc[1]), r23 = pkbf(sc[2], sc[3]);
            const float SC = 0.08838834764831845f;
            const unsigned s01 = pkbf(bflo(r01) * SC, bfhi(r01) * SC), s23 = pkbf(bflo(r23) * SC, bfhi(r23) * SC);
            sc[0] = bflo(s01); sc[1] = bfhi(s01); sc[2] = bflo(s23); sc[3] = bfhi(s23);
        }
        const float m = xrow_max(fmx(fmx(sc[0], sc[1]), fmx(sc[2], sc[3])));
#pragma unroll
        for (int r = 0; r < 4; ++r) sc[r] = expdiff(sc[r], m);
        *(LAS v2u*)(L + a_p + aw * 512) = v2u{pkbf(sc[0], sc[1]), pkbf(sc[2], sc[3])};
        const float l = xrow_sum((sc[0] + sc[1]) + (sc[2] + sc[3]));
        if (lane < 8) *(LAS v2u*)(L + a_ml + aw * 8) = v2u{fu(m), fu(l)};
        asm volatile("ds_add_u32 %0, %1 offset:%2" ::"v"(a_sig), "v"(1u), "i"(4 * F_P) : "memory");
        spin_ge_at(L_FLAG + 4 * F_P, 8);
        v2u pf[8];
        f32x4 ml[4];
#pragma unroll
        for (int t = 0; t < 4; ++t) ml[t] = *(LAS f32x4*)(L + a_ml + t * 16);
#pragma unroll
        for (int t = 0; t < 8; ++t) pf[t] = *(LAS v2u*)(L + a_p + t * 512);
        __builtin_amdgcn_sched_barrier(0);
        f32x4 ot[8];
#pragma unroll
        for (int t = 0; t < 8; ++t) ot[t] = mfma16(vf[t], pf[t], f32x4{0.f, 0.f, 0.f, 0.f});
        const float M = fmx(fmx(fmx(ml[0][0], ml[0][2]), fmx(ml[1][0], ml[1][2])), fmx(fmx(ml[2][0], ml[2][2]), fmx(ml[3][0], ml[3][2])));
        f32x4 o = {0.f, 0.f, 0.f, 0.f};
        float lt = 0.f;
#pragma unroll
        for (int t = 0; t < 8; ++t) {
            const float w = expdiff(ml[t >> 1][(t & 1) * 2], M);
            lt = fmaf(w, ml[t >> 1][(t & 1) * 2 + 1], lt);
            o += w * ot[t];
        }
        if (lj < 8) ST(pack4(o[0], o[1], o[2], o[3], tagw), dst);
        if (aw == 0 && lane < 8) ST((v4u{fu(M), tag, fu(lt), tag}), (gkv * 32 + split) * 128 + lj * 16 + WS_S2M);
    } else {
        const int gw = wave - 2;
        unsigned a0 = rbase + (((2 * gw) ^ key) << 5), a1 = rbase + (((2 * gw + 1) ^ key) << 5);
        unsigned dst = ((idx >> 3) * FF + (idx & 7) * 448 + gw * 32 + (lj >> 1) * 16 + lg * 4 + (lj & 1) * 2) * 8 + WS_SH;
        PIN(dst);
        v4u xf[2], af[2][2];
#pragma unroll
        for (int s = 0; s < 2; ++s) xf[s] = xfrag(L, L_XN + 1024 + lg * 8 + s * 64);
        wait_ge(FL + F_PROG, 96);
#pragma unroll
        for (int s = 0; s < 2; ++s) { af[s][0] = trfrag(L, L_X + a0 + s * 32768, 16384); af[s][1] = trfrag(L, L_X + a1 + s * 32768, 16384); }
        SIGNAL(F_GDONE);
        f32x4 g0 = mfma(af[0][0], xf[0], f32x4{0.f, 0.f, 0.f, 0.f}), g1 = mfma(af[0][1], xf[0], f32x4{0.f, 0.f, 0.f, 0.f});
        g0 = mfma(af[1][0], xf[1], g0); g1 = mfma(af[1][1], xf[1], g1);
        wait_ge(FL + F_PROG, 128);
        f32x4 u0 = mfma(trfrag(L, L_Y + a0, 16384), xf[0], f32x4{0.f, 0.f, 0.f, 0.f}), u1 = mfma(trfrag(L, L_Y + a1, 16384), xf[0], f32x4{0.f, 0.f, 0.f, 0.f});
        const bool hi = lj & 1;
        const f32x4 ga = lj < 2 ? g0 : g1;
        float ge = hi ? ga[2] : ga[0], go = hi ? ga[3] : ga[1];
        PIN(ge); PIN(go);
        wait_ge(FL + F_PROG, 160);
        u0 = mfma(trfrag(L, L_Y + 32768 + a0, 16384), xf[1], u0); u1 = mfma(trfrag(L, L_Y + 32768 + a1, 16384), xf[1], u1);
        const f32x4 ua = lj < 2 ? u0 : u1;
        if (lj < 4) ST(pack4(ge, hi ? ua[2] : ua[0], go, hi ? ua[3] : ua[1], tagw), dst);
        if (wave < 8) return;
    }
    {
        const int fw = wave - 8;
        const unsigned tb = (att ? L_Y : L_X) + rbase;
        unsigned a[4], dst = (((att ? 0 : 64) + ks) * 1024 + ct2 * 512 + fw * 64 + lj * 16 + lg * 4) * 4 + WS_S3, a_h = L_O + lg * 8;
#pragma unroll
        for (int t = 0; t < 4; ++t) a[t] = tb + (((4 * fw + t) ^ key) << 5);
        PIN(dst); PIN(a_h);
        v4u af[4], ag[4];
        wait_ge(FL + F_PROG, att ? 128 : 192);
#pragma unroll
        for (int t = 0; t < 4; ++t) af[t] = trfrag(L, a[t], 16384);
        BARRIER();
        const v4u h0 = xfrag(L, a_h), h1 = xfrag(L, a_h + 64);
#pragma unroll
        for (int t = 0; t < 4; ++t) ag[t] = trfrag(L, a[t] + 32768, 16384);
        __builtin_amdgcn_sched_barrier(0);
        f32x4 c[4];
#pragma unroll
        for (int t = 0; t < 4; ++t) c[t] = mfma(af[t], h0, f32x4{0.f, 0.f, 0.f, 0.f});
        __builtin_amdgcn_sched_barrier(0);
#pragma unroll
        for (int t = 0; t < 4; ++t) c[t] = mfma(ag[t], h1, c[t]);
        f32x4 cs;
#pragma unroll
        for (int k = 0; k < 4; ++k) {
            float lo = (lj & 1) ? c[1][k] : c[0][k], hi = (lj & 1) ? c[3][k] : c[2][k];
            PIN(lo); PIN(hi);
            cs[k] = (lj & 2) ? hi : lo;
        }
        if (lj < 4) ST(pack4(cs[0], cs[1], cs[2], cs[3], tagw), dst);
    }
}

extern "C" void kernel_launch(void* const* d_in, const int* in_sizes, int n_in, void* d_out, int out_size, void* d_ws, size_t ws_size,
                              hipStream_t stream) {
    (void)in_sizes; (void)n_in; (void)out_size; (void)ws_size;
    (void)hipFuncSetAttribute((const void*)fused_decode, hipFuncAttributeMaxDynamicSharedMemorySize, (int)LDS_BYTES);
    fused_decode<<<256, 1024, LDS_BYTES, stream>>>((const u16*)d_in[3], (const u16*)d_in[0], (unsigned char*)d_ws, (const u16*)d_in[1],
                                                   (const u16*)d_in[2], (const u16*)d_in[4], (const u16*)d_in[6], (const u16*)d_in[7],
                                                   (const u16*)d_in[8], (u16*)d_out);
}
```
